```python
import torch

def _softmax(x, axis=-1):
    return torch.softmax(x, dim=axis)
def _gelu(x):
    return torch.nn.functional.gelu(x, approximate="tanh")
def _silu(x):
    return torch.nn.functional.silu(x)
def _sigmoid(x):
    return torch.sigmoid(x)
def _layernorm(x, axis=-1, eps=1e-5):
    m = x.mean(dim=axis, keepdim=True)
    v = x.var(dim=axis, keepdim=True, unbiased=False)
    return (x - m) / torch.sqrt(v + eps)
def _scatter_add(tgt, idx, src, axis=0):
    return tgt.clone().index_add_(axis, idx, src)
def _segment_sum(src, seg, n):
    out = torch.zeros((n,) + src.shape[1:], dtype=src.dtype)
    return out.index_add_(0, seg, src)
def _pad(x, pw):
    flat = tuple(v for pair in reversed(pw) for v in pair)
    return torch.nn.functional.pad(x, flat)

kernel_name = 'standalone__mha_block_cached__near_ridge'

def setup_inputs():

    _w = (lambda a: (lambda t: t.to(torch.bfloat16) if t.is_floating_point() else t)(torch.as_tensor(a)))
    B, Sq, D = 16, 144, 448
    H, Sk, Dh = 16, 512, 256
    return {
        "x": _w((lambda *s: torch.randn(*s, dtype=torch.bfloat16) * 0.8)(B, Sq, D)),
        "k_cache": _w((lambda *s: torch.randn(*s, dtype=torch.bfloat16) * 0.8)(B, H, Sk, Dh)),
        "v_cache": _w((lambda *s: torch.randn(*s, dtype=torch.bfloat16) * 0.05)(B, H, Sk, Dh)),
        "wq": _w((lambda *s: torch.randn(*s, dtype=torch.bfloat16) * 0.8)(D, H * Dh) * (D ** -0.5)),
        "wo": _w((lambda *s: torch.randn(*s, dtype=torch.bfloat16) * 0.05)(H * Dh, D) * ((H * Dh) ** -0.5)),
    }

def reference(x, k_cache, v_cache, wq, wo):
    B, Sq, D = x.shape
    H, Dh = 16, 256
    q = (lambda x, *d: x.permute(*d))((x @ wq).reshape(B, Sq, H, Dh), 0, 2, 1, 3)
    scores = torch.einsum("bhqd,bhkd->bhqk", q, k_cache) * (Dh ** -0.5)
    p = _softmax(scores, axis=-1)
    o = torch.einsum("bhqk,bhkd->bhqd", p, v_cache)
    return (lambda x, *d: x.permute(*d))(o, 0, 2, 1, 3).reshape(B, Sq, H * Dh) @ wo
```

```cpp
#include <hip/hip_runtime.h>
#include <hip/hip_bf16.h>


typedef unsigned v4u __attribute__((ext_vector_type(4)));
typedef unsigned v2u __attribute__((ext_vector_type(2)));
typedef float f32x4 __attribute__((ext_vector_type(4)));
typedef float f32x2 __attribute__((ext_vector_type(2)));
typedef __bf16 bf16x8 __attribute__((ext_vector_type(8)));
typedef __bf16 bf16x2 __attribute__((ext_vector_type(2)));
#define LAS __attribute__((address_space(3)))
#define DI __device__ __forceinline__

constexpr int NB = 16, SQ = 144, DM = 448, NH = 16, SK = 512, DH = 256;
constexpr int QT = 9;
constexpr int RSRC_W3 = 0x00020000;
constexpr int AUX_NT = 2;

constexpr size_t WS_O = 0;
constexpr size_t WS_O_BYTES = (size_t)NB * NH * 8 * QT * 1024;
constexpr size_t WS_WOT = WS_O + WS_O_BYTES;
constexpr size_t WS_WOT_BYTES = (size_t)128 * 28 * 1024;
constexpr size_t WS_PART = WS_WOT + WS_WOT_BYTES;
constexpr size_t WS_PART_BYTES = (size_t)4 * 16 * 4 * 63 * 1024;

DI f32x4 mfma(v4u a, v4u b, f32x4 c) {
  return __builtin_amdgcn_mfma_f32_16x16x32_bf16(__builtin_bit_cast(bf16x8, a), __builtin_bit_cast(bf16x8, b), c, 0, 0, 0);
}
template <bool CA, bool AA, bool INIT> DI void mf(f32x4& c, const v4u& a, const v4u& b) {
  if constexpr (INIT) {
    if constexpr (CA && AA)       asm volatile("v_mfma_f32_16x16x32_bf16 %0, %1, %2, 0" : "=&a"(c) : "a"(a), "v"(b));
    else if constexpr (CA && !AA) asm volatile("v_mfma_f32_16x16x32_bf16 %0, %1, %2, 0" : "=&a"(c) : "v"(a), "v"(b));
    else if constexpr (!CA && AA) asm volatile("v_mfma_f32_16x16x32_bf16 %0, %1, %2, 0" : "=&v"(c) : "a"(a), "v"(b));
    else                          asm volatile("v_mfma_f32_16x16x32_bf16 %0, %1, %2, 0" : "=&v"(c) : "v"(a), "v"(b));
  } else {
    if constexpr (CA && AA)       asm volatile("v_mfma_f32_16x16x32_bf16 %0, %1, %2, %0" : "+a"(c) : "a"(a), "v"(b));
    else if constexpr (CA && !AA) asm volatile("v_mfma_f32_16x16x32_bf16 %0, %1, %2, %0" : "+a"(c) : "v"(a), "v"(b));
    else if constexpr (!CA && AA) asm volatile("v_mfma_f32_16x16x32_bf16 %0, %1, %2, %0" : "+v"(c) : "a"(a), "v"(b));
    else                          asm volatile("v_mfma_f32_16x16x32_bf16 %0, %1, %2, %0" : "+v"(c) : "v"(a), "v"(b));
  }
}
#define MFMA_DRAIN() do { __builtin_amdgcn_sched_barrier(0); asm volatile("s_nop 7\n\ts_nop 3" ::: "memory"); __builtin_amdgcn_sched_barrier(0); } while (0)
template <int N> struct IC { static constexpr int v = N; };
template <int B, int E, class F> DI void sfor(F&& f) { if constexpr (B < E) { f(IC<B>{}); sfor<B + 1, E>(f); } }
template <bool INA> DI float rda(float a) {
  if constexpr (INA) { float r; asm volatile("v_accvgpr_read_b32 %0, %1" : "=v"(r) : "a"(a)); return r; }
  else return a;
}
DI unsigned cvtpk(float lo, float hi) {
  f32x2 v = {lo, hi};
  bf16x2 b = __builtin_convertvector(v, bf16x2);
  return __builtin_bit_cast(unsigned, b);
}
DI float max3f(float a, float b, float c) { float r; asm("v_max3_f32 %0, %1, %2, %3" : "=v"(r) : "v"(a), "v"(b), "v"(c)); return r; }
DI float xg_max(float x) {
  auto s = __builtin_amdgcn_permlane16_swap(__float_as_uint(x), __float_as_uint(x), false, false);
  x = fmaxf(__uint_as_float(s[0]), __uint_as_float(s[1]));
  auto t = __builtin_amdgcn_permlane32_swap(__float_as_uint(x), __float_as_uint(x), false, false);
  return fmaxf(__uint_as_float(t[0]), __uint_as_float(t[1]));
}
DI float xg_sum(float x) {
  auto s = __builtin_amdgcn_permlane16_swap(__float_as_uint(x), __float_as_uint(x), false, false);
  x = __uint_as_float(s[0]) + __uint_as_float(s[1]);
  auto t = __builtin_amdgcn_permlane32_swap(__float_as_uint(x), __float_as_uint(x), false, false);
  return __uint_as_float(t[0]) + __uint_as_float(t[1]);
}
DI v4u trans(const v2u* raw, int t) {
  const unsigned sel = (t & 1) ? 0x07060302u : 0x05040100u;
  v4u a;
  if (t < 2) {
    a.x = __builtin_amdgcn_perm(raw[1].x, raw[0].x, sel);
    a.y = __builtin_amdgcn_perm(raw[3].x, raw[2].x, sel);
    a.z = __builtin_amdgcn_perm(raw[5].x, raw[4].x, sel);
    a.w = __builtin_amdgcn_perm(raw[7].x, raw[6].x, sel);
  } else {
    a.x = __builtin_amdgcn_perm(raw[1].y, raw[0].y, sel);
    a.y = __builtin_amdgcn_perm(raw[3].y, raw[2].y, sel);
    a.z = __builtin_amdgcn_perm(raw[5].y, raw[4].y, sel);
    a.w = __builtin_amdgcn_perm(raw[7].y, raw[6].y, sel);
  }
  return a;
}
DI v4u lds_rd(LAS char* base, int off) { return *(LAS v4u*)(base + off); }
DI void lds_wr(LAS char* base, int off, v4u v) { *(LAS v4u*)(base + off) = v; }
#define SB() __builtin_amdgcn_sched_barrier(0)
DI void bar() {
  asm volatile("s_waitcnt lgkmcnt(0)" ::: "memory");
  __builtin_amdgcn_s_barrier();
  asm volatile("" ::: "memory");
}

constexpr int WQ_D = 5;
constexpr int PD = 3;
constexpr int K_AGPR = 28;
constexpr int K_PRO = 12;
constexpr int k_cum1(int s) { return s < 0 ? K_PRO : (s < 9 ? K_PRO + 3 * (s + 1) : ((K_PRO + 27 + 5 * (s - 8)) > 64 ? 64 : (K_PRO + 27 + 5 * (s - 8)))); }
constexpr int v_cum2(int n) { return n < 0 ? 0 : (n < 8 ? 1 * (n + 1) : 8 + 6 * (n - 7)); }
constexpr int V_SM1 = 64;
constexpr int v_cum_sm2(int qt) { return qt < 0 ? V_SM1 : V_SM1 + 3 * (qt + 1); }
constexpr int V_AHEAD = 11;
constexpr int v_cum3(int S) {
  return S < 0 ? v_cum_sm2(8) : ((8 * (S + 1 + V_AHEAD) > 128) ? 128 : (8 * (S + 1 + V_AHEAD) < v_cum_sm2(8) ? v_cum_sm2(8) : 8 * (S + 1 + V_AHEAD)));
}

constexpr int LDS_STAT = 147456;
constexpr int LDS_BYTES = LDS_STAT + 2 * 144 * 16;

__global__ __launch_bounds__(256, 1) void k_attn(const char* __restrict__ x, const char* __restrict__ kc, const char* __restrict__ vc,
                                                 const char* __restrict__ wq, const char* __restrict__ wo, char* __restrict__ ws) {
  __shared__ __attribute__((aligned(16))) char lds_raw[LDS_BYTES];
  LAS char* lds = (LAS char*)lds_raw;
  const int bid = blockIdx.x;
  const int xc = bid & 7, jj = bid >> 3;
  const int b = 8 * (xc >> 2) + (jj >> 2), h = 4 * (xc & 3) + (jj & 3);
  const int bh = b * NH + h;
  const int lane = threadIdx.x & 63;
  const int w = __builtin_amdgcn_readfirstlane(threadIdx.x >> 6);
  const int i = lane & 15, g = lane >> 4;
  const int l16 = lane * 16;

  const __amdgpu_buffer_rsrc_t rx = __builtin_amdgcn_make_buffer_rsrc((void*)(x + (size_t)b * SQ * DM * 2), (short)0, SQ * DM * 2, RSRC_W3);
  const __amdgpu_buffer_rsrc_t rk = __builtin_amdgcn_make_buffer_rsrc((void*)(kc + (size_t)bh * SK * DH * 2 + w * 65536), (short)0, 65536, RSRC_W3);
  const __amdgpu_buffer_rsrc_t rv = __builtin_amdgcn_make_buffer_rsrc((void*)(vc + (size_t)bh * SK * DH * 2 + w * 128), (short)0, SK * DH * 2 - w * 128, RSRC_W3);
  const __amdgpu_buffer_rsrc_t rq = __builtin_amdgcn_make_buffer_rsrc((void*)(wq + h * 512 + w * 128), (short)0, DM * 8192 - (h * 512 + w * 128), RSRC_W3);

  const int vx = i * 896 + g * 16;
  const int vk = i * 512 + g * 16;
  const int vv = g * 2048 + i * 8;
  const int vq = g * 65536 + i * 8;

  v4u kr[64];
  v2u vr[128];
  v2u qr[14 * 8];
  auto ldK = [&](int n) { const int G = n >> 5, s = (n >> 2) & 7, kt = (G << 2) + (n & 3);
    kr[n] = __builtin_amdgcn_raw_buffer_load_b128(rk, vk + s * 64, kt * 8192, AUX_NT); };
  auto ldV = [&](int n) { const int S = n >> 3, j = n & 7;
    vr[n] = __builtin_amdgcn_raw_buffer_load_b64(rv, vv + (j & 3) * 512, S * 16384 + (j >> 2) * 8192, AUX_NT); };
  auto ldQ = [&](int s) {
#pragma unroll
    for (int j = 0; j < 8; ++j) qr[s * 8 + j] = __builtin_amdgcn_raw_buffer_load_b64(rq, vq, (32 * s + j) * 8192, 0); };

  {
    const unsigned lbase = (unsigned)(unsigned long long)lds;
#pragma unroll
    for (int n = 0; n < 32; ++n) {
      const int f = 4 * n + w;
      const int s = f / 9, qt = f - 9 * s;
      const unsigned m0v = lbase + f * 1024;
      const unsigned so = qt * 16 * 896 + s * 64;
      if (n < 31 || w < 2)
        asm volatile("s_mov_b32 m0, %0\n\ts_nop 0\n\tbuffer_load_dwordx4 %1, %2, %3 offen lds" :: "s"(m0v), "v"(vx), "s"(rx), "s"(so) : "memory");
    }
  }
  SB();
  const int gw = bid * 4 + w;
  const int pblk = gw % 7, pks = gw / 7;
  v2u wr[8];
  {
    const __amdgpu_buffer_rsrc_t ro = __builtin_amdgcn_make_buffer_rsrc((void*)wo, (short)0, gw < 896 ? 4096 * 896 : 0, RSRC_W3);
    const int vo = g * 8 * 896 + i * 8, so = pks * 32 * 896 + pblk * 128;
#pragma unroll
    for (int j = 0; j < 8; ++j) wr[j] = __builtin_amdgcn_raw_buffer_load_b64(ro, vo + j * 896, so, 0);
  }
#pragma unroll
  for (int s = 0; s < WQ_D; ++s) ldQ(s);
#pragma unroll
  for (int n = 0; n < K_PRO; ++n) ldK(n);
  SB();
  static_assert(8 + 8 * WQ_D + K_PRO <= 63, "vmcnt");
  asm volatile("s_waitcnt vmcnt(%0)" :: "i"(8 + 8 * WQ_D + K_PRO) : "memory");
  bar();
  {
    const __amdgpu_buffer_rsrc_t rt = __builtin_amdgcn_make_buffer_rsrc((void*)(ws + WS_WOT), (short)0, gw < 896 ? (int)WS_WOT_BYTES : 0, RSRC_W3);
    const int nt = 4 * pblk + (i >> 2);
#pragma unroll
    for (int t = 0; t < 4; ++t) {
      const v4u a = trans(wr, t);
      const int off = ((pks * 28 + nt) * 64 + g * 16 + ((4 * i + t) & 15)) * 16;
      __builtin_amdgcn_raw_buffer_store_b128(a, rt, off, 0, 0);
    }
  }
  SB();

  f32x4 acc[4][QT];
  {
    v4u bf[14 * QT];
    v4u af[14][4];
#pragma unroll
    for (int n = 0; n < PD; ++n) bf[n] = lds_rd(lds, n * 1024 + l16);
#pragma unroll
    for (int t = 0; t < 4; ++t) af[0][t] = trans(&qr[0], t);
    SB();
    sfor<0, 14 * QT>([&](auto I) {
      constexpr int idx = decltype(I)::v, s = idx / QT, qt = idx % QT;
      if constexpr (idx + PD < 14 * QT) bf[idx + PD] = lds_rd(lds, (idx + PD) * 1024 + l16);
      if constexpr (qt == 0 && s + WQ_D < 14) ldQ(s + WQ_D);
      if constexpr (qt >= 1 && qt - 1 < k_cum1(s) - k_cum1(s - 1)) ldK(k_cum1(s - 1) + qt - 1);
      if constexpr (qt >= 4 && qt < 8 && s + 1 < 14) af[s + 1][qt - 4] = trans(&qr[(s + 1) * 8], qt - 4);
      sfor<0, 4>([&](auto T) { constexpr int t = decltype(T)::v; mf<true, false, s == 0>(acc[t][qt], af[s][t], bf[idx]); });
      SB();
    });
    MFMA_DRAIN();
  }
  bar();
  {
    const int so = (2 * w + (g >> 1)) * QT * 1024 + (2 * (g & 1)) * 256 + i * 16;
#pragma unroll
    for (int qt = 0; qt < QT; ++qt) {
      v4u lo, hi;
      lo.x = cvtpk(acc[0][qt][0], acc[1][qt][0]); lo.y = cvtpk(acc[2][qt][0], acc[3][qt][0]);
      lo.z = cvtpk(acc[0][qt][1], acc[1][qt][1]); lo.w = cvtpk(acc[2][qt][1], acc[3][qt][1]);
      hi.x = cvtpk(acc[0][qt][2], acc[1][qt][2]); hi.y = cvtpk(acc[2][qt][2], acc[3][qt][2]);
      hi.z = cvtpk(acc[0][qt][3], acc[1][qt][3]); hi.w = cvtpk(acc[2][qt][3], acc[3][qt][3]);
      lds_wr(lds, so + qt * 1024, lo);
      lds_wr(lds, so + qt * 1024 + 256, hi);
    }
  }
  bar();

  f32x4 sc[8][QT];
  {
    v4u bf[16 * QT];
#pragma unroll
    for (int n = 0; n < PD; ++n) bf[n] = lds_rd(lds, n * 1024 + l16);
    SB();
    sfor<0, 16 * QT>([&](auto I) {
      constexpr int idx = decltype(I)::v, n = idx / QT, qt = idx % QT, G = n >> 3, s = n & 7;
      if constexpr (idx + PD < 16 * QT) bf[idx + PD] = lds_rd(lds, ((idx + PD) % (8 * QT)) * 1024 + l16);
      if constexpr (qt >= 1 && qt - 1 < v_cum2(n) - v_cum2(n - 1)) ldV(v_cum2(n - 1) + qt - 1);
      sfor<0, 4>([&](auto T) { constexpr int t = decltype(T)::v;
        mf<(G == 0 || qt < 7), (n * 4 + t < K_AGPR), s == 0>(sc[4 * G + t][qt], kr[n * 4 + t], bf[idx]); });
      SB();
    });
    MFMA_DRAIN();
  }

  const float cs = 0.0625f * 1.44269504088896340736f;
  sfor<0, QT>([&](auto I) {
    constexpr int qt = decltype(I)::v;
    float m = -INFINITY, m2 = -INFINITY;
    sfor<0, 4>([&](auto T) { constexpr int t = decltype(T)::v;
      m = max3f(m, rda<true>(sc[t][qt][0]), rda<true>(sc[t][qt][1]));
      m = max3f(m, rda<true>(sc[t][qt][2]), rda<true>(sc[t][qt][3]));
      m2 = max3f(m2, rda<(qt < 7)>(sc[4 + t][qt][0]), rda<(qt < 7)>(sc[4 + t][qt][1]));
      m2 = max3f(m2, rda<(qt < 7)>(sc[4 + t][qt][2]), rda<(qt < 7)>(sc[4 + t][qt][3]));
    });
    m = xg_max(max3f(m, m2, m2));
    if (g == 0) *(LAS float*)(lds + LDS_STAT + (qt * 16 + i) * 16 + w * 4) = m;
    if constexpr (qt < V_SM1 - v_cum2(15)) ldV(v_cum2(15) + qt);
    SB();
  });
  bar();
  sfor<0, QT>([&](auto I) {
    constexpr int qt = decltype(I)::v;
    const f32x4 mm = *(LAS f32x4*)(lds + LDS_STAT + (qt * 16 + i) * 16);
    const float m = max3f(max3f(mm[0], mm[1], mm[2]), mm[3], mm[3]);
    const float nm = -m * cs;
    float sum0 = 0.f, sum1 = 0.f;
    sfor<0, 4>([&](auto U) { constexpr int u = decltype(U)::v; constexpr bool ina = (u < 2 || qt < 7);
      float p[8];
#pragma unroll
      for (int r = 0; r < 4; ++r) {
        p[r] = __builtin_amdgcn_exp2f(__builtin_fmaf(rda<ina>(sc[2 * u][qt][r]), cs, nm));
        p[4 + r] = __builtin_amdgcn_exp2f(__builtin_fmaf(rda<ina>(sc[2 * u + 1][qt][r]), cs, nm));
        sum0 += p[r]; sum1 += p[4 + r];
      }
      v4u pk = {cvtpk(p[0], p[1]), cvtpk(p[2], p[3]), cvtpk(p[4], p[5]), cvtpk(p[6], p[7])};
      lds_wr(lds, ((4 * w + u) * QT + qt) * 1024 + l16, pk);
    });
    const float sm = xg_sum(sum0 + sum1);
    if (g == 0) *(LAS float*)(lds + LDS_STAT + 2304 + (qt * 16 + i) * 16 + w * 4) = sm;
    sfor<v_cum_sm2(qt - 1), v_cum_sm2(qt)>([&](auto E) { ldV(decltype(E)::v); });
    SB();
  });
  bar();

  {
    v4u bf[16 * QT];
    v4u af[16][4];
#pragma unroll
    for (int n = 0; n < PD; ++n) bf[n] = lds_rd(lds, n * 1024 + l16);
#pragma unroll
    for (int t = 0; t < 4; ++t) af[0][t] = trans(&vr[0], t);
    SB();
    sfor<0, 16 * QT>([&](auto I) {
      constexpr int idx = decltype(I)::v, S = idx / QT, qt = idx % QT;
      if constexpr (idx + PD < 16 * QT) bf[idx + PD] = lds_rd(lds, (idx + PD) * 1024 + l16);
      if constexpr (qt < 8 && qt < v_cum3(S) - v_cum3(S - 1)) ldV(v_cum3(S - 1) + qt);
      if constexpr (qt >= 4 && qt < 8 && S + 1 < 16) af[S + 1][qt - 4] = trans(&vr[(S + 1) * 8], qt - 4);
      sfor<0, 4>([&](auto T) { constexpr int t = decltype(T)::v; mf<true, false, S == 0>(acc[t][qt], af[S][t], bf[idx]); });
      SB();
    });
    MFMA_DRAIN();
  }
  {
    const __amdgpu_buffer_rsrc_t ro = __builtin_amdgcn_make_buffer_rsrc((void*)(ws + WS_O + (size_t)bh * 8 * QT * 1024), (short)0, 8 * QT * 1024, RSRC_W3);
    const int so = (2 * w + (g >> 1)) * QT * 1024 + (2 * (g & 1)) * 256 + i * 16;
#pragma unroll
    for (int qt = 0; qt < QT; ++qt) {
      const f32x4 ss = *(LAS f32x4*)(lds + LDS_STAT + 2304 + (qt * 16 + i) * 16);
      const float rl = 1.0f / ((ss[0] + ss[1]) + (ss[2] + ss[3]));
      v4u lo, hi;
      lo.x = cvtpk(acc[0][qt][0] * rl, acc[1][qt][0] * rl); lo.y = cvtpk(acc[2][qt][0] * rl, acc[3][qt][0] * rl);
      lo.z = cvtpk(acc[0][qt][1] * rl, acc[1][qt][1] * rl); lo.w = cvtpk(acc[2][qt][1] * rl, acc[3][qt][1] * rl);
      hi.x = cvtpk(acc[0][qt][2] * rl, acc[1][qt][2] * rl); hi.y = cvtpk(acc[2][qt][2] * rl, acc[3][qt][2] * rl);
      hi.z = cvtpk(acc[0][qt][3] * rl, acc[1][qt][3] * rl); hi.w = cvtpk(acc[2][qt][3] * rl, acc[3][qt][3] * rl);
      __builtin_amdgcn_raw_buffer_store_b128(lo, ro, so + qt * 1024, 0, 0);
      __builtin_amdgcn_raw_buffer_store_b128(hi, ro, so + qt * 1024 + 256, 0, 0);
    }
  }
}

constexpr int OP_AHEAD = 3;
__global__ __launch_bounds__(256, 1) void k_oproj(char* __restrict__ ws) {
  __shared__ __attribute__((aligned(16))) char lds_raw[96 * 1024];
  LAS char* lds = (LAS char*)lds_raw;
  const int bid = blockIdx.x;
  const int xc = bid & 7, jj = bid >> 3;
  const int b = 8 * (xc >> 2) + (jj >> 2), a = xc & 3, ns = jj & 3;
  const int lane = threadIdx.x & 63;
  const int w = __builtin_amdgcn_readfirstlane(threadIdx.x >> 6);
  const int l16 = lane * 16;
  const int h = 4 * a + w;
  const __amdgpu_buffer_rsrc_t ro = __builtin_amdgcn_make_buffer_rsrc((void*)(ws + WS_O + (size_t)(b * NH + h) * 8 * QT * 1024), (short)0, 8 * QT * 1024, RSRC_W3);
  const __amdgpu_buffer_rsrc_t rt = __builtin_amdgcn_make_buffer_rsrc((void*)(ws + WS_WOT + (size_t)(h * 8 * 28 + ns * 7) * 1024), (short)0, (7 * 28 + 7) * 1024, RSRC_W3);
  v4u of[8][QT], wf[8][7];
  auto ld = [&](int s) {
#pragma unroll
    for (int nt = 0; nt < 7; ++nt) wf[s][nt] = __builtin_amdgcn_raw_buffer_load_b128(rt, l16, (s * 28 + nt) * 1024, 0);
#pragma unroll
    for (int qt = 0; qt < QT; ++qt) of[s][qt] = __builtin_amdgcn_raw_buffer_load_b128(ro, l16, (s * QT + qt) * 1024, 0);
  };
  f32x4 acc[7][QT];
#pragma unroll
  for (int s = 0; s < OP_AHEAD; ++s) ld(s);
  SB();
#pragma unroll
  for (int s = 0; s < 8; ++s) {
#pragma unroll
    for (int qt = 0; qt < QT; ++qt) {
#pragma unroll
      for (int nt = 0; nt < 7; ++nt) {
        const f32x4 c = s == 0 ? (f32x4){0.f, 0.f, 0.f, 0.f} : acc[nt][qt];
        acc[nt][qt] = mfma(wf[s][nt], of[s][qt], c);
      }
    }
    SB();
    if (s + OP_AHEAD < 8) ld(s + OP_AHEAD);
    SB();
  }
  const __amdgpu_buffer_rsrc_t rp = __builtin_amdgcn_make_buffer_rsrc((void*)(ws + WS_PART + (size_t)((a * 16 + b) * 4 + ns) * 63 * 1024), (short)0, 63 * 1024, RSRC_W3);
#pragma unroll
  for (int r = 0; r < 2; ++r) {
#pragma unroll
    for (int tl = 0; tl < 32; ++tl) {
      const int idx = 32 * r + tl;
      if (idx < 63) {
        const int own = idx & 3;
        if (w != own) {
          const int slot = (w - own - 1) & 3;
          lds_wr(lds, (tl * 3 + slot) * 1024 + l16, __builtin_bit_cast(v4u, acc[idx / 9][idx % 9]));
        }
      }
    }
    bar();
#pragma unroll
    for (int tl = 0; tl < 32; ++tl) {
      const int idx = 32 * r + tl;
      if (idx < 63) {
        const int own = idx & 3;
        if (w == own) {
          f32x4 v = acc[idx / 9][idx % 9];
#pragma unroll
          for (int sl = 0; sl < 3; ++sl) v += __builtin_bit_cast(f32x4, lds_rd(lds, (tl * 3 + sl) * 1024 + l16));
          __builtin_amdgcn_raw_buffer_store_b128(__builtin_bit_cast(v4u, v), rp, l16, idx * 1024, 0);
        }
      }
    }
    if (r == 0) bar();
  }
}

__global__ __launch_bounds__(256) void k_red(const char* __restrict__ ws, char* __restrict__ out) {
  const int gwv = blockIdx.x * 4 + (threadIdx.x >> 6);
  const int lane = threadIdx.x & 63;
  const int idx = gwv % 63, bn = gwv / 63;
  const int b = bn >> 2, ns = bn & 3, nt = idx / 9, qt = idx % 9;
  const char* p = ws + WS_PART + ((size_t)bn * 63 + idx) * 1024 + lane * 16;
  f32x4 v = *(const f32x4*)p;
  v += *(const f32x4*)(p + (size_t)1 * 64 * 63 * 1024);
  v += *(const f32x4*)(p + (size_t)2 * 64 * 63 * 1024);
  v += *(const f32x4*)(p + (size_t)3 * 64 * 63 * 1024);
  const int q = qt * 16 + (lane & 15), n = ns * 112 + nt * 16 + 4 * (lane >> 4);
  v2u o = {cvtpk(v[0], v[1]), cvtpk(v[2], v[3])};
  *(v2u*)(out + ((size_t)(b * SQ + q) * DM + n) * 2) = o;
}

extern "C" void kernel_launch(void* const* d_in, const int* in_sizes, int n_in,
                              void* d_out, int out_size, void* d_ws, size_t ws_size,
                              hipStream_t stream) {
  const char* x = (const char*)d_in[0];
  const char* kc = (const char*)d_in[1];
  const char* vc = (const char*)d_in[2];
  const char* wq = (const char*)d_in[3];
  const char* wo = (const char*)d_in[4];
  char* ws = (char*)d_ws;
  k_attn<<<256, 256, 0, stream>>>(x, kc, vc, wq, wo, ws);
  k_oproj<<<256, 256, 0, stream>>>(ws);
  k_red<<<1008, 256, 0, stream>>>(ws, (char*)d_out);
}
```

```cpp
#include <hip/hip_runtime.h>
#include <hip/hip_bf16.h>
#include <utility>

typedef unsigned v4u __attribute__((ext_vector_type(4)));
typedef unsigned v2u __attribute__((ext_vector_type(2)));
typedef float f32x4 __attribute__((ext_vector_type(4)));
typedef float f32x2 __attribute__((ext_vector_type(2)));
typedef __bf16 bf16x8 __attribute__((ext_vector_type(8)));
typedef __bf16 bf16x2 __attribute__((ext_vector_type(2)));
#define LAS __attribute__((address_space(3)))
#define DI __device__ __forceinline__
constexpr int NB = 16, SQ = 144, DM = 448, NH = 16, SK = 512, DH = 256;
constexpr int QT = 9;
constexpr int RSRC_W3 = 0x00020000;
constexpr int AUX_NT = 2;

constexpr size_t WS_O = 0;
constexpr size_t WS_O_BYTES = (size_t)NB * NH * 8 * QT * 1024;
constexpr size_t WS_WOT = WS_O + WS_O_BYTES;
constexpr size_t WS_WOT_BYTES = (size_t)128 * 28 * 1024;
constexpr size_t WS_SLAB = WS_WOT + WS_WOT_BYTES;
constexpr size_t WS_SLAB_BYTES = (size_t)16 * 4 * 4 * 4 * 24 * 1024;
constexpr size_t WS_FW = WS_SLAB + WS_SLAB_BYTES;
#define FSTR 128
#define WSTR 8
#define PSLEEP 1
constexpr size_t WS_FO = WS_FW + 4096;
constexpr size_t WS_FP = WS_FO + 256 * FSTR;
constexpr int AUX_SC1 = 16;
#define KVPOL "nt"
#define AUX_ST 16
#define LDPOL "sc1"
constexpr unsigned SPIN_CAP = 1u << 16;
typedef unsigned long long u64;
typedef __attribute__((address_space(1))) u64 gu64;
extern "C" __device__ u64 amd_dispatch_id() __asm("llvm.amdgcn.dispatch.id");

#define ALL_AGPR "a0", "a1", "a2", "a3", "a4", "a5", "a6", "a7", "a8", "a9", "a10", "a11", "a12", "a13", "a14", "a15", "a16", "a17", "a18", "a19", "a20", "a21", "a22", "a23", "a24", "a25", "a26", "a27", "a28", "a29", "a30", "a31", "a32", "a33", "a34", "a35", "a36", "a37", "a38", "a39", "a40", "a41", "a42", "a43", "a44", "a45", "a46", "a47", "a48", "a49", "a50", "a51", "a52", "a53", "a54", "a55", "a56", "a57", "a58", "a59", "a60", "a61", "a62", "a63", "a64", "a65", "a66", "a67", "a68", "a69", "a70", "a71", "a72", "a73", "a74", "a75", "a76", "a77", "a78", "a79", "a80", "a81", "a82", "a83", "a84", "a85", "a86", "a87", "a88", "a89", "a90", "a91", "a92", "a93", "a94", "a95", "a96", "a97", "a98", "a99", "a100", "a101", "a102", "a103", "a104", "a105", "a106", "a107", "a108", "a109", "a110", "a111", "a112", "a113", "a114", "a115", "a116", "a117", "a118", "a119", "a120", "a121", "a122", "a123", "a124", "a125", "a126", "a127", "a128", "a129", "a130", "a131", "a132", "a133", "a134", "a135", "a136", "a137", "a138", "a139", "a140", "a141", "a142", "a143", "a144", "a145", "a146", "a147", "a148", "a149", "a150", "a151", "a152", "a153", "a154", "a155", "a156", "a157", "a158", "a159", "a160", "a161", "a162", "a163", "a164", "a165", "a166", "a167", "a168", "a169", "a170", "a171", "a172", "a173", "a174", "a175", "a176", "a177", "a178", "a179", "a180", "a181", "a182", "a183", "a184", "a185", "a186", "a187", "a188", "a189", "a190", "a191", "a192", "a193", "a194", "a195", "a196", "a197", "a198", "a199", "a200", "a201", "a202", "a203", "a204", "a205", "a206", "a207", "a208", "a209", "a210", "a211", "a212", "a213", "a214", "a215", "a216", "a217", "a218", "a219", "a220", "a221", "a222", "a223", "a224", "a225", "a226", "a227", "a228", "a229", "a230", "a231", "a232", "a233", "a234", "a235", "a236", "a237", "a238", "a239", "a240", "a241", "a242", "a243", "a244", "a245", "a246", "a247", "a248", "a249", "a250", "a251", "a252", "a253", "a254", "a255"
template <int C, bool INIT> DI void mfa(const v4u& a, const v4u& b) {
  if constexpr ((C & 15) == 0) {
    if constexpr (INIT) asm volatile("v_mfma_f32_16x16x32_bf16 a[%c0:%c1], %2, %3, 0" :: "i"(C), "i"(C + 3), "v"(a), "v"(b) : ALL_AGPR);
    else                asm volatile("v_mfma_f32_16x16x32_bf16 a[%c0:%c1], %2, %3, a[%c0:%c1]" :: "i"(C), "i"(C + 3), "v"(a), "v"(b) : ALL_AGPR);
  } else {
    if constexpr (INIT) asm volatile("v_mfma_f32_16x16x32_bf16 a[%c0:%c1], %2, %3, 0" :: "i"(C), "i"(C + 3), "v"(a), "v"(b));
    else                asm volatile("v_mfma_f32_16x16x32_bf16 a[%c0:%c1], %2, %3, a[%c0:%c1]" :: "i"(C), "i"(C + 3), "v"(a), "v"(b));
  }
}
template <int C, int A, bool INIT> DI void mfaa(const v4u& b) {
  if constexpr ((C & 15) == 0) {
    if constexpr (INIT) asm volatile("v_mfma_f32_16x16x32_bf16 a[%c0:%c1], a[%c2:%c3], %4, 0" :: "i"(C), "i"(C + 3), "i"(A), "i"(A + 3), "v"(b) : ALL_AGPR);
    else                asm volatile("v_mfma_f32_16x16x32_bf16 a[%c0:%c1], a[%c2:%c3], %4, a[%c0:%c1]" :: "i"(C), "i"(C + 3), "i"(A), "i"(A + 3), "v"(b) : ALL_AGPR);
  } else {
    if constexpr (INIT) asm volatile("v_mfma_f32_16x16x32_bf16 a[%c0:%c1], a[%c2:%c3], %4, 0" :: "i"(C), "i"(C + 3), "i"(A), "i"(A + 3), "v"(b));
    else                asm volatile("v_mfma_f32_16x16x32_bf16 a[%c0:%c1], a[%c2:%c3], %4, a[%c0:%c1]" :: "i"(C), "i"(C + 3), "i"(A), "i"(A + 3), "v"(b));
  }
}
template <bool INIT> DI void mfv(f32x4& c, const v4u& a, const v4u& b) {
  if constexpr (INIT) asm volatile("v_mfma_f32_16x16x32_bf16 %0, %1, %2, 0" : "=&v"(c) : "v"(a), "v"(b));
  else                asm volatile("v_mfma_f32_16x16x32_bf16 %0, %1, %2, %0" : "+v"(c) : "v"(a), "v"(b));
}
template <int A, int OFF> DI void lds_rd_a(int addr) {
  asm volatile("ds_read_b128 a[%c0:%c1], %2 offset:%3" :: "i"(A), "i"(A + 3), "v"(addr), "i"(OFF) : "memory");
}
#define MFMA_DRAIN() do { __builtin_amdgcn_sched_barrier(0); asm volatile("s_nop 7\n\ts_nop 3" ::: "memory"); __builtin_amdgcn_sched_barrier(0); } while (0)
template <int N> struct IC { static constexpr int v = N; };
template <int B, class F, int... I> DI void sfor_(F& f, std::integer_sequence<int, I...>) { (f(IC<B + I>{}), ...); }
template <int B, int E, class F> DI void sfor(F&& f) { if constexpr (B < E) sfor_<B>(f, std::make_integer_sequence<int, E - B>{}); }
template <int A0, int A1> DI void max_tiles_a(float& m0, float& m1) {
  float t0, t1, t2, t3, t4, t5, t6, t7;
  asm volatile("v_accvgpr_read_b32 %2, a%c10\n\tv_accvgpr_read_b32 %3, a%c11\n\tv_accvgpr_read_b32 %6, a%c14\n\tv_accvgpr_read_b32 %7, a%c15\n\t"
               "v_accvgpr_read_b32 %4, a%c12\n\tv_accvgpr_read_b32 %5, a%c13\n\tv_accvgpr_read_b32 %8, a%c16\n\tv_accvgpr_read_b32 %9, a%c17\n\t"
               "v_max3_f32 %0, %0, %2, %3\n\tv_max3_f32 %1, %1, %6, %7\n\tv_max3_f32 %0, %0, %4, %5\n\tv_max3_f32 %1, %1, %8, %9"
               : "+v"(m0), "+v"(m1), "=&v"(t0), "=&v"(t1), "=&v"(t2), "=&v"(t3), "=&v"(t4), "=&v"(t5), "=&v"(t6), "=&v"(t7)
               : "i"(A0), "i"(A0 + 1), "i"(A0 + 2), "i"(A0 + 3), "i"(A1), "i"(A1 + 1), "i"(A1 + 2), "i"(A1 + 3) : ALL_AGPR);
}
DI void max_tiles_v(float& m0, float& m1, const f32x4& c0, const f32x4& c1) {
  asm volatile("v_max3_f32 %0, %0, %2, %3\n\tv_max3_f32 %1, %1, %6, %7\n\tv_max3_f32 %0, %0, %4, %5\n\tv_max3_f32 %1, %1, %8, %9"
               : "+v"(m0), "+v"(m1)
               : "v"(c0[0]), "v"(c0[1]), "v"(c0[2]), "v"(c0[3]), "v"(c1[0]), "v"(c1[1]), "v"(c1[2]), "v"(c1[3]));
}
template <int A0, int A1> DI void rd_tiles_a(f32x2* t) {
  float t0, t1, t2, t3, t4, t5, t6, t7;
  asm volatile("v_accvgpr_read_b32 %0, a%c8\n\tv_accvgpr_read_b32 %1, a%c9\n\tv_accvgpr_read_b32 %2, a%c10\n\tv_accvgpr_read_b32 %3, a%c11\n\t"
               "v_accvgpr_read_b32 %4, a%c12\n\tv_accvgpr_read_b32 %5, a%c13\n\tv_accvgpr_read_b32 %6, a%c14\n\tv_accvgpr_read_b32 %7, a%c15"
               : "=v"(t0), "=v"(t1), "=v"(t2), "=v"(t3), "=v"(t4), "=v"(t5), "=v"(t6), "=v"(t7)
               : "i"(A0), "i"(A0 + 1), "i"(A0 + 2), "i"(A0 + 3), "i"(A1), "i"(A1 + 1), "i"(A1 + 2), "i"(A1 + 3) : ALL_AGPR);
  t[0] = (f32x2){t0, t1}; t[1] = (f32x2){t2, t3}; t[2] = (f32x2){t4, t5}; t[3] = (f32x2){t6, t7};
}
DI void rd_tiles_v(f32x2* t, const f32x4& c0, const f32x4& c1) {
  t[0] = (f32x2){c0[0], c0[1]}; t[1] = (f32x2){c0[2], c0[3]}; t[2] = (f32x2){c1[0], c1[1]}; t[3] = (f32x2){c1[2], c1[3]};
}
constexpr int areg(int kt, int qt) { return (kt >> 2) * 144 + 16 * qt + 4 * (kt & 3); }
constexpr int kreg(int u, int e) { return 144 + 16 * u + 4 * e; }
DI unsigned cvtpk(float lo, float hi) {
  f32x2 v = {lo, hi};
  bf16x2 b = __builtin_convertvector(v, bf16x2);
  return __builtin_bit_cast(unsigned, b);
}
DI float max3f(float a, float b, float c) { float r; asm("v_max3_f32 %0, %1, %2, %3" : "=v"(r) : "v"(a), "v"(b), "v"(c)); return r; }
DI float xg_max(float x) {
  auto s = __builtin_amdgcn_permlane16_swap(__float_as_uint(x), __float_as_uint(x), false, false);
  x = fmaxf(__uint_as_float(s[0]), __uint_as_float(s[1]));
  auto t = __builtin_amdgcn_permlane32_swap(__float_as_uint(x), __float_as_uint(x), false, false);
  return fmaxf(__uint_as_float(t[0]), __uint_as_float(t[1]));
}
DI float xg_sum(float x) {
  auto s = __builtin_amdgcn_permlane16_swap(__float_as_uint(x), __float_as_uint(x), false, false);
  x = __uint_as_float(s[0]) + __uint_as_float(s[1]);
  auto t = __builtin_amdgcn_permlane32_swap(__float_as_uint(x), __float_as_uint(x), false, false);
  return __uint_as_float(t[0]) + __uint_as_float(t[1]);
}
template <bool MAX> DI float xg4(float a, float b, float c, float d) {
  auto op = [](unsigned x, unsigned y) { return MAX ? max3f(__uint_as_float(x), __uint_as_float(y), __uint_as_float(y)) : __uint_as_float(x) + __uint_as_float(y); };
  auto s = __builtin_amdgcn_permlane16_swap(__float_as_uint(a), __float_as_uint(b), false, false);
  auto t = __builtin_amdgcn_permlane16_swap(__float_as_uint(c), __float_as_uint(d), false, false);
  const float ab = op(s[0], s[1]), cd = op(t[0], t[1]);
  auto u = __builtin_amdgcn_permlane32_swap(__float_as_uint(ab), __float_as_uint(cd), false, false);
  return op(u[0], u[1]);
}
DI v4u trans(const v2u* raw, int t) {
  const unsigned sel = (t & 1) ? 0x07060302u : 0x05040100u;
  v4u a;
  if (t < 2) {
    a.x = __builtin_amdgcn_perm(raw[1].x, raw[0].x, sel);
    a.y = __builtin_amdgcn_perm(raw[3].x, raw[2].x, sel);
    a.z = __builtin_amdgcn_perm(raw[5].x, raw[4].x, sel);
    a.w = __builtin_amdgcn_perm(raw[7].x, raw[6].x, sel);
  } else {
    a.x = __builtin_amdgcn_perm(raw[1].y, raw[0].y, sel);
    a.y = __builtin_amdgcn_perm(raw[3].y, raw[2].y, sel);
    a.z = __builtin_amdgcn_perm(raw[5].y, raw[4].y, sel);
    a.w = __builtin_amdgcn_perm(raw[7].y, raw[6].y, sel);
  }
  return a;
}
DI v4u lds_rd(LAS char* base, int off) { return *(LAS v4u*)(base + off); }
DI void lds_wr(LAS char* base, int off, v4u v) { *(LAS v4u*)(base + off) = v; }
#define SB() __builtin_amdgcn_sched_barrier(0)
DI void bar() {
  asm volatile("s_waitcnt lgkmcnt(0)" ::: "memory");
  __builtin_amdgcn_s_barrier();
  asm volatile("" ::: "memory");
}

enum : unsigned char { OP_NONE = 0, OP_BRD, OP_PERM, OP_WQL, OP_VL, OP_KD, OP_KR, OP_WQW, OP_WVW, OP_WKW, OP_XB, OP_XD, OP_WOL, OP_WOS,
                       OP_MX, OP_LBAR, OP_MRD, OP_SUM, OP_WBAR, OP_WFL, OP_WFW, OP_WTL, OP_XCH, OP_MXW };
struct Op { unsigned char kind; unsigned char b; short a; short n; };
constexpr int MAXOP = 4;
#define PDV 3
constexpr int PD = PDV;
constexpr int WQ_PRO = 3;
#define P1_SEQ "XXWWWWKWWKXWWKWXWKWXORKRK.RK.RK"
#define WQ_NT false
#define X_NT false
#define K_PRO 1
#define V_G0 68
#define V_G1 124
#define QW_UNITS 0
#define KD13 0
#define KD14 0
#define KD15 0
#define MIDK 8
#define AUX_OUT 0
#define VSP 3
#define P2_RSP 20
constexpr int K_AGPR_UNITS = 7;
constexpr int NS1 = 14 * 36, NS2 = 16 * 36, NS3 = 12 * 36;
constexpr int V_P2 = 64;
constexpr int v_cum_sm(int qt) { return qt < 0 ? V_P2 : (V_P2 + 2 * (qt + 1) > 82 ? 82 : V_P2 + 2 * (qt + 1)); }
#define V_AHEAD 10
constexpr int WT_P3 = 46;
#define WT_EARLY 46
#define WT_G0 53
#define WT_GN 46
constexpr int WT_PRE = 28;
struct Prog {
  Op p1[NS1][MAXOP], p2[NS2][MAXOP], p3[NS3][MAXOP];
  short n_x, n_wq0, n_v0, n_ws, qw0, n_qw[4];
};
struct Bld {
  Prog P{};
  int load1[NS1] = {}, load2[NS2] = {}, load3[NS3] = {};
  constexpr int put(int ph, int lo, int hi, int cost, Op op) {
    Op (*p)[MAXOP] = ph == 1 ? P.p1 : ph == 2 ? P.p2 : P.p3;
    int* ld = ph == 1 ? load1 : ph == 2 ? load2 : load3;
    const int ns = ph == 1 ? NS1 : ph == 2 ? NS2 : NS3;
    if (hi > ns) hi = ns;
    int best = -1;
    for (int sl = lo; sl < hi; ++sl) if (p[sl][MAXOP - 1].kind == OP_NONE && (best < 0 || ld[sl] < ld[best])) best = sl;
    if (best < 0) throw 1;
    for (int k = 0; k < MAXOP; ++k) if (p[best][k].kind == OP_NONE) { p[best][k] = op; break; }
    ld[best] += cost;
    return best;
  }
};
constexpr Prog build_prog() {
  Bld B{};
  for (int ph = 1; ph <= 3; ++ph) {
    const int ng = (ph == 1 ? NS1 : ph == 2 ? NS2 : NS3) / 4;
    int* ld = ph == 1 ? B.load1 : ph == 2 ? B.load2 : B.load3;
    for (int gI = 0; gI < ng; ++gI) {
      ld[4 * gI] += 1;
      if (gI + PD < ng) B.put(ph, 4 * gI + 1, 4 * gI + 2, 1, Op{OP_BRD, 0, (short)(gI + PD), 0});
    }
  }
  auto perms = [&](int ph, int s) {
    const int sl = B.put(ph, (s - 1) * 36 + 2, (s - 1) * 36 + 4, 1, Op{ph == 1 ? OP_WQW : OP_WVW, 0, (short)s, 0});
    for (int e = 0; e < 16; ++e) {
      const int lo = (s - 1) * 36 + 2 * e + 1;
      B.put(ph, lo > sl ? lo : sl + 1, (s - 1) * 36 + 2 * e + 5 < s * 36 - 2 ? (s - 1) * 36 + 2 * e + 5 : s * 36 - 2, 1, Op{OP_PERM, (unsigned char)e, (short)s, 0});
    }
  };
  auto perms_q = [&](int s) {
    const int sl = B.put(1, (s - 1) * 36 + 2, (s - 1) * 36 + 4, 1, Op{OP_WQW, 0, (short)s, 0});
    int last = sl;
    for (int e = 0; e < 16; ++e) {
      int lo = (s - 1) * 36 + (3 * e) / 2 + 1; if (lo <= sl) lo = sl + 1;
      const int p = B.put(1, lo, lo + 3, 1, Op{OP_PERM, (unsigned char)e, (short)s, 0});
      if (p > last) last = p;
    }
    const int x0 = B.put(1, last + 1, last + 3, 9, Op{OP_XCH, 0, (short)s, 0});
    if (B.put(1, x0 + 2, x0 + 4, 9, Op{OP_XCH, 1, (short)s, 0}) >= s * 36 - 1) throw 12;
  };
  auto kread = [&](int ph, int slot, int u) {
    const int sl = B.put(ph, slot, slot + 2, 1, Op{OP_WKW, 0, (short)u, 0});
    int last = sl;
    for (int e = 0; e < 4; ++e) last = B.put(ph, last + 1, last + 5, 1, Op{OP_KR, (unsigned char)e, (short)u, 0});
    return last;
  };
  constexpr int XB_SLOT[5] = {2 * 36 + 7, 4 * 36 + 7, 7 * 36 + 19, 9 * 36 + 19, 11 * 36 + 19};
  constexpr int XB_LAND[5] = {3, -1, 4, 5, 6};
  for (int k = 0; k < 5; ++k) B.put(1, XB_SLOT[k], XB_SLOT[k] + 1, 2, Op{OP_XB, 0, (short)XB_LAND[k], 0});
  B.put(1, 13 * 36 + 2, 13 * 36 + 3, 24, Op{OP_WOS, 0, 0, 0});
  int nk1 = 0;
  int nd1 = K_PRO;
  {
    constexpr const char* seq = P1_SEQ;
    int sl = 2, xn = 2, wn = WQ_PRO, rdy[16] = {};
    for (int c = 0; seq[c]; ++c) {
      if (seq[c] == 'W') { for (int j = 0; j < 4; ++j) sl = B.put(1, sl, sl + 2, j == 0 ? 3 : 1, Op{OP_WQL, (unsigned char)j, (short)wn, 0}) + VSP; ++wn; }
      else if (seq[c] == 'X') { if (xn >= 4 && sl <= XB_SLOT[xn - 4]) sl = XB_SLOT[xn - 4] + 1;
        for (int k = 0; k < 5; ++k) sl = B.put(1, sl, sl + 2, 4, Op{OP_XD, (unsigned char)k, (short)xn, 0}) + VSP; ++xn; }
      else if (seq[c] == 'K') { if (nd1 >= 5) { if (nk1 <= nd1 - 5) throw 9; if (sl < rdy[nd1 - 5]) sl = rdy[nd1 - 5]; }
        sl = B.put(1, sl, sl + 2, 6, Op{OP_KD, 0, (short)nd1, 0}) + 2 * VSP; ++nd1; }
      else if (seq[c] == 'R') { rdy[nk1] = kread(1, sl, nk1) + 16; ++nk1; }
      else if (seq[c] == 'O') { for (int j = 0; j < 8; ++j) sl = B.put(1, sl, sl + 2, 1, Op{OP_WOL, (unsigned char)j, 0, 0}) + VSP; }
      else if (seq[c] == '.') sl += 18;
    }
    if (wn != 14 || xn != 7) throw 10;
  }
  if (nk1 < 4 || nk1 > 11) throw 6;
  for (int s = 1; s < 14; ++s) perms_q(s);
  if (nd1 != nk1 + 5 && QW_UNITS) throw 14;
  B.P.qw0 = (short)nk1;
  nk1 += QW_UNITS; nd1 += QW_UNITS;
  for (int u = nd1; u < nk1 + 5; ++u) B.put(2, 2 + 8 * (u - nd1), 4 + 8 * (u - nd1), 6, Op{OP_KD, 0, (short)u, 0});
  int vstart = 0;
  for (int u = nk1; u < 16; ++u) {
    const int slot = u <= 10 ? 6 + P2_RSP * (u - nk1) : (u - 4) * 36 + 2;
    const int last = kread(2, slot, u);
    if (last >= ((u >> 3) * 8 + ((u >> 2) & 1) * 4) * 36 - 2) throw 5;
    if (u + 5 < 16) { int lo = last + 16; const int late = u + 5 == 13 ? KD13 : u + 5 == 14 ? KD14 : u + 5 == 15 ? KD15 : 0; if (lo < late) lo = late;
      vstart = B.put(2, lo, lo + 3, 6, Op{OP_KD, 0, (short)(u + 5), 0}); }
  }
  {
    const int g0 = (vstart / 4 + 1 > V_G0 ? vstart / 4 + 1 : V_G0), g1 = V_G1;
    for (int n = 0; n < 48; ++n) { const int gI = g0 + (n * (g1 - g0)) / 48; B.put(2, 4 * gI + 2, 4 * gI + 4, (n & 3) == 0 ? 2 : 1, Op{OP_VL, 0, (short)n, 0}); }
    for (int n = 48; n < V_P2; ++n) { const int gI = g1 + ((n - 48) * (144 - g1 - 1)) / 16; B.put(2, 4 * gI + 2, 4 * gI + 4, (n & 3) == 0 ? 2 : 1, Op{OP_VL, 0, (short)n, 0}); }
  }
  for (int qt = 0; qt < QT; ++qt) B.put(2, 8 * 36 + 10 + 10 * qt, 8 * 36 + 14 + 10 * qt, 13, Op{OP_MX, 0, (short)qt, 0});
  for (int k = 0; k < 3; ++k) B.put(2, 8 * 36 + 46 + 40 * k - (k == 2 ? 30 : 0), 8 * 36 + 49 + 40 * k - (k == 2 ? 30 : 0), 9, Op{OP_MXW, 0, (short)k, 0});
  B.put(2, 11 * 36 + 20, 11 * 36 + 21, 3, Op{OP_LBAR, 0, 0, 0});
  for (int qt = 0; qt < QT; ++qt) B.put(2, 12 * 36 + 2 + 12 * qt, 12 * 36 + 6 + 12 * qt, 6, Op{OP_MRD, 0, (short)qt, 0});
  {
    int n = v_cum_sm(8);
    for (int S = 0; S < 12; ++S) {
      int e = 0;
      while (n < 128 && n < 8 * (S + 1 + V_AHEAD)) { B.put(3, S * 36 + 4 * e + 2, S * 36 + 4 * e + 4, (n & 3) == 0 ? 2 : 1, Op{OP_VL, 0, (short)n, 0}); ++n; ++e; }
    }
    B.put(3, 34, 36, 3, Op{OP_WFL, 0, 0, 0});
    B.put(3, 4 * 36 + 30, 4 * 36 + 31, 6, Op{OP_WFW, 0, 0, 0});
    for (int k = 0; k < WT_EARLY; ++k) {
      const int gI = WT_G0 + (k * WT_GN) / WT_EARLY;
      B.put(3, 4 * gI + 2, 4 * gI + 4, (k & 1) ? 1 : 2, Op{OP_WTL, 0, (short)k, 0});
    }
    for (int k = 0; k < 3; ++k) B.put(3, 10 + 8 * k, 12 + 8 * k, 9, Op{OP_SUM, 0, (short)k, 0});
    B.put(3, 10 * 36 + 20, 10 * 36 + 21, 3, Op{OP_LBAR, 0, 0, 0});
    for (int S = 1; S < 11; ++S) perms(3, S);
    for (int S = 11; S < 16; ++S) {
      const int lo = 9 * 36 + 18 + (S - 11) * 17, hi = lo + 17;
      const int sl = B.put(3, lo, lo + 2, 1, Op{OP_WVW, 0, (short)S, 0});
      for (int e = 0; e < 16; ++e) B.put(3, sl + 1, hi, 1, Op{OP_PERM, (unsigned char)e, (short)S, 0});
    }
  }
  int cnt = 0;
  int sq_ws = 0, sq_wf = 0;
  int sq_x[7] = {}, sq_wo = 0, sq_wq[14] = {}, sq_k[16] = {}, sq_v[16] = {};
  auto nn = [&](int seq) { const int n = cnt - seq; return (short)(n > 63 ? 63 : n); };
  for (int s = 0; s < WQ_PRO; ++s) { cnt += 4; sq_wq[s] = cnt; }
  for (int u = 0; u < K_PRO; ++u) { cnt += 4; sq_k[u] = cnt; }
  B.P.n_x = nn(0);
  B.P.n_wq0 = nn(sq_wq[0]);
  auto walk = [&](Op (*p)[MAXOP], int ns) {
    for (int sl = 0; sl < ns; ++sl) for (int k = 0; k < MAXOP; ++k) {
      Op& o = p[sl][k];
      if (o.kind == OP_WQL) { ++cnt; sq_wq[o.a] = cnt; }
      else if (o.kind == OP_VL) { ++cnt; sq_v[o.a >> 3] = cnt; }
      else if (o.kind == OP_WOL) { ++cnt; sq_wo = cnt; }
      else if (o.kind == OP_XD) { ++cnt; sq_x[o.a] = cnt; }
      else if (o.kind == OP_KD) { cnt += 4; sq_k[o.a] = cnt; }
      else if (o.kind == OP_WQW) { if (!sq_wq[o.a]) throw 8; o.n = nn(sq_wq[o.a]); }
      else if (o.kind == OP_WVW) { if (!sq_v[o.a]) throw 8; o.n = nn(sq_v[o.a]); }
      else if (o.kind == OP_WKW) { if (!sq_k[o.a]) throw 8; o.n = nn(sq_k[o.a]); }
      else if (o.kind == OP_XB) { if (o.a >= 0 && !sq_x[o.a]) throw 8; o.n = o.a >= 0 ? nn(sq_x[o.a]) : (short)63; }
      else if (o.kind == OP_WOS) { o.n = nn(sq_wo); cnt += 4; sq_ws = cnt; }
      else if (o.kind == OP_WFL) { ++cnt; sq_wf = cnt; }
      else if (o.kind == OP_WFW) o.n = nn(sq_wf);
      else if (o.kind == OP_WTL) ++cnt;
    }
  };
  walk(B.P.p1, NS1);
  for (int j = 0; j < QW_UNITS; ++j) { B.P.n_qw[j] = nn(sq_k[B.P.qw0 + j]); cnt += 4; sq_k[B.P.qw0 + j + 5] = cnt; }
  B.P.n_ws = nn(sq_ws); ++cnt;
  walk(B.P.p2, NS2);
  for (int n = V_P2; n < v_cum_sm(8); ++n) { ++cnt; sq_v[n >> 3] = cnt; }
  B.P.n_v0 = nn(sq_v[0]);
  walk(B.P.p3, NS3);
  return B.P;
}
constexpr Prog PG = build_prog();

constexpr int X_PANEL = 18432;
constexpr int LDS_K = 4 * X_PANEL;
constexpr int LDS_WMAX = LDS_K + 5 * 16384;
constexpr int LDS_FLAG = LDS_WMAX + 2304;
constexpr int LDS_WSUM = LDS_FLAG + 256;
constexpr int LDS_DUMMY = LDS_WSUM + 2304 + 256;
constexpr int LDS_BYTES = 163840;
static_assert(LDS_DUMMY + 1024 <= LDS_BYTES, "lds");
constexpr int kslot(int u) { return LDS_K + (u % 5) * 4096; }
constexpr float SUM_LIMIT = 65536.f;

template <int N> DI void wait8(v2u* r) {
  asm volatile("s_waitcnt vmcnt(%8)" : "+v"(r[0]), "+v"(r[1]), "+v"(r[2]), "+v"(r[3]), "+v"(r[4]), "+v"(r[5]), "+v"(r[6]), "+v"(r[7]) : "i"(N));
}
template <int N> DI void waitm() { asm volatile("s_waitcnt vmcnt(%0)" :: "i"(N) : "memory"); }
template <int IMM, bool NT> DI void ld64(v2u& d, int voff, const __amdgpu_buffer_rsrc_t& r, int soff) {
  if constexpr (NT) asm volatile("buffer_load_dwordx2 %0, %1, %2, %3 offen offset:%4 " KVPOL : "=v"(d) : "v"(voff), "s"(r), "s"(soff), "i"(IMM));
  else              asm volatile("buffer_load_dwordx2 %0, %1, %2, %3 offen offset:%4" : "=v"(d) : "v"(voff), "s"(r), "s"(soff), "i"(IMM));
}
DI void dma1(unsigned m0v, int v, const __amdgpu_buffer_rsrc_t& r, int soff) {
  if constexpr (X_NT) asm volatile("s_mov_b32 m0, %0\n\ts_nop 0\n\tbuffer_load_dwordx4 %1, %2, %3 offen nt lds" :: "s"(m0v), "v"(v), "s"(r), "s"(soff) : "memory");
  else asm volatile("s_mov_b32 m0, %0\n\ts_nop 0\n\tbuffer_load_dwordx4 %1, %2, %3 offen lds" :: "s"(m0v), "v"(v), "s"(r), "s"(soff) : "memory");
}
DI void touch(unsigned m0v, int v, const __amdgpu_buffer_rsrc_t& r, int soff) {
  asm volatile("s_mov_b32 m0, %0\n\ts_nop 0\n\tbuffer_load_dword %1, %2, %3 offen sc1 lds" :: "s"(m0v), "v"(v), "s"(r), "s"(soff) : "memory");
}
DI void dma4(unsigned m0v, int v0, int v1, int v2, int v3, const __amdgpu_buffer_rsrc_t& r, int soff) {
  asm volatile("s_mov_b32 m0, %0\n\ts_nop 0\n\t"
               "buffer_load_dwordx4 %1, %5, %6 offen " KVPOL " lds\n\t"
               "buffer_load_dwordx4 %2, %5, %6 offen offset:1024 " KVPOL " lds\n\t"
               "buffer_load_dwordx4 %3, %5, %6 offen offset:2048 " KVPOL " lds\n\t"
               "buffer_load_dwordx4 %4, %5, %6 offen offset:3072 " KVPOL " lds"
               :: "s"(m0v), "v"(v0), "v"(v1), "v"(v2), "v"(v3), "s"(r), "s"(soff) : "memory");
}
template <int A> DI void ld128_a(int voff, const __amdgpu_buffer_rsrc_t& r, int soff) {
  asm volatile("buffer_load_dwordx4 a[%c0:%c1], %2, %3, %4 offen " LDPOL :: "i"(A), "i"(A + 3), "v"(voff), "s"(r), "s"(soff));
}
DI void ld128_v(v4u& d, int voff, const __amdgpu_buffer_rsrc_t& r, int soff) {
  asm volatile("buffer_load_dwordx4 %0, %1, %2, %3 offen " LDPOL : "=v"(d) : "v"(voff), "s"(r), "s"(soff));
}
DI void dma1_sc1(unsigned m0v, int v, const __amdgpu_buffer_rsrc_t& r, int soff) {
  asm volatile("s_mov_b32 m0, %0\n\ts_nop 0\n\tbuffer_load_dwordx4 %1, %2, %3 offen " LDPOL " lds" :: "s"(m0v), "v"(v), "s"(r), "s"(soff) : "memory");
}
DI void st_flag(char* p, u64 tag) { __hip_atomic_store((gu64*)p, tag, __ATOMIC_RELAXED, __HIP_MEMORY_SCOPE_AGENT); }
DI void poll_flag(const char* p, u64 tag) {
  for (unsigned spin = 0; spin < SPIN_CAP; ++spin) {
    const u64 v = __hip_atomic_load((gu64*)p, __ATOMIC_RELAXED, __HIP_MEMORY_SCOPE_AGENT);
    if (__builtin_amdgcn_readfirstlane((unsigned)(v ^ tag) | (unsigned)((v ^ tag) >> 32)) == 0) break;
    __builtin_amdgcn_s_sleep(PSLEEP);
  }
  asm volatile("" ::: "memory");
}
DI void ld_flags(v2u& d, int voff, const __amdgpu_buffer_rsrc_t& r) {
  asm volatile("buffer_load_dwordx2 %0, %1, %2, 0 offen sc1" : "=v"(d) : "v"(voff), "s"(r));
}
template <int N> DI void wait_flags(v2u& d) { asm volatile("s_waitcnt vmcnt(%1)" : "+v"(d) : "i"(N) : "memory"); }
DI void poll_flags(const char* p, int off, bool active, u64 tag) {
  for (unsigned spin = 0; spin < SPIN_CAP; ++spin) {
    const u64 v = __hip_atomic_load((gu64*)(p + off), __ATOMIC_RELAXED, __HIP_MEMORY_SCOPE_AGENT);
    if (__builtin_amdgcn_ballot_w64(active && v != tag) == 0) break;
    __builtin_amdgcn_s_sleep(PSLEEP);
  }
  asm volatile("" ::: "memory");
}
template <int N> DI void wait4(v4u* r) {
  asm volatile("s_waitcnt vmcnt(%4)" : "+v"(r[0]), "+v"(r[1]), "+v"(r[2]), "+v"(r[3]) : "i"(N));
}
DI float fmac_k(float x, float c) {
  float r; asm("v_fmamk_f32 %0, %1, 0x3db8aa3b, %2" : "=v"(r) : "v"(x), "v"(c)); return r;
}
DI int fresh_lane() {
  int l; asm volatile("v_mbcnt_lo_u32_b32 %0, -1, 0\n\tv_mbcnt_hi_u32_b32 %0, -1, %0" : "=v"(l)); return l;
}
DI unsigned perm1(const v2u* raw, int t, int d) {
  const unsigned sel = (t & 1) ? 0x07060302u : 0x05040100u;
  unsigned r;
  asm volatile("v_perm_b32 %0, %1, %2, %3" : "=v"(r) : "v"(t < 2 ? raw[2 * d + 1].x : raw[2 * d + 1].y), "v"(t < 2 ? raw[2 * d].x : raw[2 * d].y), "s"(sel));
  return r;
}
DI unsigned permq(const v4u* raw, int e) {
  const int c8 = e >> 1, hh = e & 1;
  const unsigned sel = (c8 & 1) ? 0x07060302u : 0x05040100u;
  unsigned r;
  asm volatile("v_perm_b32 %0, %1, %2, %3" : "=v"(r) : "v"(raw[2 * hh + 1][c8 >> 1]), "v"(raw[2 * hh][c8 >> 1]), "s"(sel));
  return r;
}
template <int HI> DI void xch(v4u* af, const unsigned* tq) {
  const unsigned* keep = tq + (HI ? 8 : 0);
  const unsigned* send = tq + (HI ? 0 : 8);
  unsigned o[8];
  asm volatile("s_mov_b64 vcc, %24\n\ts_nop 1\n\t"
               "v_cndmask_b32_dpp %0, %8, %16, vcc row_ror:8 row_mask:0xf bank_mask:0xf\n\t"
               "v_cndmask_b32_dpp %1, %9, %17, vcc row_ror:8 row_mask:0xf bank_mask:0xf\n\t"
               "v_cndmask_b32_dpp %2, %10, %18, vcc row_ror:8 row_mask:0xf bank_mask:0xf\n\t"
               "v_cndmask_b32_dpp %3, %11, %19, vcc row_ror:8 row_mask:0xf bank_mask:0xf\n\t"
               "v_cndmask_b32_dpp %4, %12, %20, vcc row_ror:8 row_mask:0xf bank_mask:0xf\n\t"
               "v_cndmask_b32_dpp %5, %13, %21, vcc row_ror:8 row_mask:0xf bank_mask:0xf\n\t"
               "v_cndmask_b32_dpp %6, %14, %22, vcc row_ror:8 row_mask:0xf bank_mask:0xf\n\t"
               "v_cndmask_b32_dpp %7, %15, %23, vcc row_ror:8 row_mask:0xf bank_mask:0xf"
               : "=&v"(o[0]), "=&v"(o[1]), "=&v"(o[2]), "=&v"(o[3]), "=&v"(o[4]), "=&v"(o[5]), "=&v"(o[6]), "=&v"(o[7])
               : "v"(send[0]), "v"(send[1]), "v"(send[2]), "v"(send[3]), "v"(send[4]), "v"(send[5]), "v"(send[6]), "v"(send[7]),
                 "v"(keep[0]), "v"(keep[1]), "v"(keep[2]), "v"(keep[3]), "v"(keep[4]), "v"(keep[5]), "v"(keep[6]), "v"(keep[7]),
                 "s"(HI ? 0xFF00FF00FF00FF00ull : 0x00FF00FF00FF00FFull)
               : "vcc");
#pragma unroll
  for (int t = 0; t < 4; ++t) { af[t][2 * HI] = o[2 * t]; af[t][2 * HI + 1] = o[2 * t + 1]; }
}
template <bool NT> DI void ld128p(v4u& d, int voff, const __amdgpu_buffer_rsrc_t& r, int soff) {
  asm volatile("buffer_load_dwordx4 %0, %1, %2, %3 offen" : "=v"(d) : "v"(voff), "s"(r), "s"(soff));
}
DI void trans4(v4u* af, const v2u* raw) {
#pragma unroll
  for (int e = 0; e < 16; ++e) af[e >> 2][e & 3] = perm1(raw, e >> 2, e & 3);
  asm volatile("s_nop 1");
}

__global__ __launch_bounds__(256, 1) void k_attn(const char* __restrict__ x, const char* __restrict__ kc, const char* __restrict__ vc,
                                                 const char* __restrict__ wq, const char* __restrict__ wo, char* __restrict__ ws, char* __restrict__ out) {
#if defined(__HIP_DEVICE_COMPILE__)
  __shared__ __attribute__((aligned(16))) char lds_raw[LDS_BYTES];
  LAS char* lds = (LAS char*)lds_raw;
  const int bid = blockIdx.x;
  const int xc = bid & 7, jj = bid >> 3;
  const int b = 8 * (xc >> 2) + (jj >> 2), h = 4 * (xc & 3) + (jj & 3);
  const int bh = b * NH + h;
  const int lane = threadIdx.x & 63;
  const int w = __builtin_amdgcn_readfirstlane(threadIdx.x >> 6);
  const int i = lane & 15, g = lane >> 4;
  const int l16 = lane * 16;
  v4u code_pf = {0, 0, 0, 0};
  const u64 tag = (((u64)__builtin_amdgcn_dispatch_ptr() >> 6) ^ (amd_dispatch_id() << 41)) ^ 0x5bd1e9955bd1e995ull;
  {
    unsigned long long pc; unsigned len;
    asm volatile("s_getpc_b64 %0\n.Lattn_code_begin:\n\ts_mov_b32 %1, .Lattn_code_end-.Lattn_code_begin" : "=s"(pc), "=s"(len));
    const __amdgpu_buffer_rsrc_t rc = __builtin_amdgcn_make_buffer_rsrc((void*)(pc & ~15ull), (short)0, len > 0x20000u ? 0 : (int)(len & ~15u), RSRC_W3);
    v4u dummy;
    asm volatile("buffer_load_dwordx4 %0, %1, %2, 0 offen" : "=v"(dummy) : "v"((jj * 4 + w) * 1024 + l16), "s"(rc));
    code_pf = dummy;
  }

  const __amdgpu_buffer_rsrc_t rx = __builtin_amdgcn_make_buffer_rsrc((void*)(x + (size_t)b * SQ * DM * 2), (short)0, SQ * DM * 2, RSRC_W3);
  const __amdgpu_buffer_rsrc_t rk = __builtin_amdgcn_make_buffer_rsrc((void*)(kc + (size_t)bh * SK * DH * 2 + w * 65536), (short)0, 65536, RSRC_W3);
  const __amdgpu_buffer_rsrc_t rv = __builtin_amdgcn_make_buffer_rsrc((void*)(vc + (size_t)bh * SK * DH * 2 + w * 128), (short)0, SK * DH * 2 - w * 128, RSRC_W3);
  const char* wqb = wq + h * 512 + w * 128;
  const unsigned lbase = (unsigned)(unsigned long long)lds;

  const int vx = (lane >> 3) * 896 + (((lane & 7) ^ (lane >> 4)) ^ (4 * (w & 1))) * 16;
  auto dmaX1 = [&](auto P, auto K) { constexpr int p = decltype(P)::v, k = decltype(K)::v;
    const int u0 = w ^ (2 * (p & 1));
    if constexpr (k < 4) dma1(lbase + (p & 3) * X_PANEL + (u0 + 4 * k) * 1024, vx, rx, (u0 + 4 * k) * 7168 + p * 128);
    else dma1(u0 < 2 ? lbase + (p & 3) * X_PANEL + (u0 + 16) * 1024 : lbase + LDS_DUMMY, vx, rx, (u0 < 2 ? u0 + 16 : u0) * 7168 + p * 128); };
  auto dmaX = [&](auto P) { sfor<0, 5>([&](auto K) { dmaX1(P, K); }); };
  dmaX(IC<0>{}); dmaX(IC<1>{});
  SB();
  const int vv = g * 2048 + i * 8;
  const int vq = (8 * g + 4 * (i >> 3)) * 8192 + (i & 7) * 16;
  int kv[4], ka[4];
#pragma unroll
  for (int m = 0; m < 4; ++m) {
    kv[m] = 1024 * m + g * 512 + (4 * ((i >> 2) ^ m) + ((i & 3) ^ g)) * 16;
    ka[m] = LDS_K + w * 20480 + i * 256 + (4 * (m ^ (i >> 2)) + (g ^ (i & 3))) * 16;
  }
  v4u kr[64];
  v2u vr[128];
  v4u qr[14 * 4];
  auto ldQ = [&](auto S, auto J) { constexpr int s = decltype(S)::v, j = decltype(J)::v;
    const __amdgpu_buffer_rsrc_t rq = __builtin_amdgcn_make_buffer_rsrc((void*)(wqb + s * 262144), (short)0, 31 * 8192 + 128, RSRC_W3);
    ld128p<false>(qr[s * 4 + j], vq, rq, j * 8192); };
  auto ldV = [&](auto N) { constexpr int n = decltype(N)::v, S = n >> 3, j = n & 7;
    ld64<(j & 3) * 512, true>(vr[n], vv, rv, S * 16384 + (j >> 2) * 8192); };
  auto dmaK = [&](auto U) { constexpr int u = decltype(U)::v, kt = 4 * (u >> 3) + (u & 3), hf = (u >> 2) & 1;
    const unsigned m0v = lbase + kslot(u) + w * 20480;
    dma4(m0v, kv[0], kv[1], kv[2], kv[3], rk, kt * 8192 + hf * 256); };
  auto rdK = [&](auto U, auto E) { constexpr int u = decltype(U)::v, e = decltype(E)::v;
    constexpr int n = ((u >> 3) * 8 + ((u >> 2) & 1) * 4 + e) * 4 + (u & 3);
    if constexpr (u < K_AGPR_UNITS) lds_rd_a<kreg(u, e), kslot(u) - LDS_K>(ka[e]);
    else kr[n] = lds_rd(lds, ka[e] + (kslot(u) - LDS_K)); };

  const int gw = bid * 4 + w;
  const int pblk = gw % 7, pks = gw / 7;
  v2u wr[8];
  const __amdgpu_buffer_rsrc_t rwo = __builtin_amdgcn_make_buffer_rsrc((void*)wo, (short)0, gw < 896 ? 4096 * 896 : 0, RSRC_W3);
  const __amdgpu_buffer_rsrc_t rwt = __builtin_amdgcn_make_buffer_rsrc((void*)(ws + WS_WOT), (short)0, gw < 896 ? (int)WS_WOT_BYTES : 0, RSRC_W3);
  auto ldWo = [&](auto J) { constexpr int j = decltype(J)::v;
    ld64<(j & 3) * 896, false>(wr[j], g * 8 * 896 + i * 8, rwo, pks * 32 * 896 + pblk * 128 + (j >> 2) * 4 * 896); };
  auto stWo = [&]() {
    const int sc = 3 * X_PANEL + w * 4096;
    const int off = sc + ((i >> 2) * 64 + g * 16 + ((4 * i) & 15)) * 16;
#pragma unroll
    for (int t = 0; t < 4; ++t) lds_wr(lds, off + t * 16, trans(wr, t));
#pragma unroll
    for (int t = 0; t < 4; ++t) __builtin_amdgcn_raw_buffer_store_b128(lds_rd(lds, sc + t * 1024 + l16), rwt, l16, (pks * 28 + 4 * pblk + t) * 1024, AUX_ST); };
  sfor<0, WQ_PRO>([&](auto S) { sfor<0, 4>([&](auto J) { ldQ(S, J); }); });
  sfor<0, K_PRO>([&](auto U) { dmaK(U); });
  if (threadIdx.x == 0) *(LAS unsigned*)(lds + LDS_FLAG) = 0u;
  SB();
  waitm<PG.n_x>();
  asm volatile("" :: "v"(code_pf));
  bar();
  {
    v4u bf[14 * QT];
    v4u af[14][4];
    const int xa0 = i * 128 + (g ^ ((i >> 1) & 7)) * 16;
    auto rdX = [&](auto I) { constexpr int idx = decltype(I)::v, s = idx / QT, qt = idx % QT;
      bf[idx] = lds_rd(lds, ((s & 1) ? (xa0 ^ 64) : xa0) + ((s >> 1) & 3) * X_PANEL + qt * 2048); };
    sfor<0, PD>([&](auto I) { rdX(I); });
    unsigned tq[14][16];
    wait4<PG.n_wq0>(&qr[0]);
#pragma unroll
    for (int e = 0; e < 16; ++e) tq[0][e] = permq(&qr[0], e);
    xch<0>(af[0], tq[0]);
    xch<1>(af[0], tq[0]);
    asm volatile("s_nop 1");
    SB();
    sfor<0, NS1>([&](auto M) {
      constexpr int m = decltype(M)::v, idx = m >> 2, s = idx / QT, qt = idx % QT, t = m & 3;
      sfor<0, MAXOP>([&](auto K) {
        constexpr Op o = PG.p1[m][decltype(K)::v];
        if constexpr (o.kind == OP_BRD) rdX(IC<o.a>{});
        else if constexpr (o.kind == OP_PERM) tq[o.a][o.b] = permq(&qr[o.a * 4], o.b);
        else if constexpr (o.kind == OP_XCH) xch<o.b>(af[o.a], tq[o.a]);
        else if constexpr (o.kind == OP_WQL) ldQ(IC<o.a>{}, IC<o.b>{});
        else if constexpr (o.kind == OP_WQW) wait4<o.n>(&qr[o.a * 4]);
        else if constexpr (o.kind == OP_KD) dmaK(IC<o.a>{});
        else if constexpr (o.kind == OP_WKW) waitm<o.n>();
        else if constexpr (o.kind == OP_KR) rdK(IC<o.a>{}, IC<o.b>{});
        else if constexpr (o.kind == OP_XD) dmaX1(IC<o.a>{}, IC<o.b>{});
        else if constexpr (o.kind == OP_WOS) { wait8<o.n>(wr); stWo(); }
        else if constexpr (o.kind == OP_WOL) ldWo(IC<o.b>{});
        else if constexpr (o.kind == OP_XB) { waitm<o.n>(); asm volatile("s_barrier" ::: "memory"); }
      });
      mfa<areg(t, qt), s == 0>(af[s][t], bf[idx]);
      SB();
    });
    MFMA_DRAIN();
  }
  bar();
  {
    const int so = (2 * w + (g & 1)) * QT * 1024 + i * 16 + (g >> 1) * 8;
    sfor<0, QT>([&](auto I) {
      constexpr int qt = decltype(I)::v;
      f32x2 c[8];
      rd_tiles_a<areg(0, qt), areg(1, qt)>(c);
      rd_tiles_a<areg(2, qt), areg(3, qt)>(c + 4);
#pragma unroll
      for (int r = 0; r < 4; ++r) {
        const v2u q2 = {cvtpk(c[r >> 1][r & 1], c[2 + (r >> 1)][r & 1]), cvtpk(c[4 + (r >> 1)][r & 1], c[6 + (r >> 1)][r & 1])};
        *(LAS v2u*)(lds + so + qt * 1024 + r * 256) = q2;
      }
      if constexpr (qt % 4 == 0 && qt / 4 < QW_UNITS) { waitm<PG.n_qw[qt / 4]>(); sfor<0, 4>([&](auto E) { rdK(IC<PG.qw0 + qt / 4>{}, E); }); }
      if constexpr (qt % 4 == 2 && qt / 4 < QW_UNITS) { asm volatile("s_waitcnt lgkmcnt(0)" ::: "memory"); dmaK(IC<PG.qw0 + qt / 4 + 5>{}); }
    });
  }
  waitm<PG.n_ws>();
  bar();
  if (lane == 0) st_flag(ws + WS_FW + bid * 8, tag);
  const float cs = 0.0625f * 1.44269504088896340736f;
  f32x4 sv[4][2];
  float nm[QT];
  {
    v4u bf[16 * QT];
    int qb1 = l16 + 65536;
    asm volatile("" : "+v"(qb1));
    auto rdQ = [&](auto I) { constexpr int idx = decltype(I)::v % (8 * QT); bf[decltype(I)::v] = lds_rd(lds, (idx < 64 ? l16 : qb1) + (idx & 63) * 1024); };
    sfor<0, PD>([&](auto I) { rdQ(I); });
    SB();
    sfor<0, NS2>([&](auto M) {
      constexpr int m = decltype(M)::v, idx = m >> 2, n = idx / QT, qt = idx % QT, t = m & 3, G = n >> 3, s = n & 7;
      sfor<0, MAXOP>([&](auto K) {
        constexpr Op o = PG.p2[m][decltype(K)::v];
        if constexpr (o.kind == OP_BRD) rdQ(IC<o.a>{});
        else if constexpr (o.kind == OP_VL) ldV(IC<o.a>{});
        else if constexpr (o.kind == OP_KD) dmaK(IC<o.a>{});
        else if constexpr (o.kind == OP_WKW) waitm<o.n>();
        else if constexpr (o.kind == OP_KR) rdK(IC<o.a>{}, IC<o.b>{});
        else if constexpr (o.kind == OP_MX) {
          float m0 = -INFINITY, m1 = -INFINITY;
          max_tiles_a<areg(0, o.a), areg(1, o.a)>(m0, m1);
          nm[o.a] = max3f(m0, m1, m1);
        }
        else if constexpr (o.kind == OP_MXW) {
          if constexpr (o.a < 2) *(LAS float*)(lds + LDS_WMAX + o.a * 1024 + l16 + w * 4) = xg4<true>(nm[4 * o.a], nm[4 * o.a + 1], nm[4 * o.a + 2], nm[4 * o.a + 3]);
          else *(LAS float*)(lds + LDS_WMAX + 2048 + (l16 & 255) + w * 4) = xg_max(nm[8]);
        }
        else if constexpr (o.kind == OP_LBAR) bar();
        else if constexpr (o.kind == OP_MRD) {
          const f32x4 mm = *(LAS f32x4*)(lds + LDS_WMAX + o.a * 256 + (l16 & 255));
          nm[o.a] = -cs * max3f(max3f(mm[0], mm[1], mm[2]), mm[3], mm[3]);
        }
      });
      constexpr int unit = G * 8 + (s >> 2) * 4 + t;
      if constexpr (G == 1 && qt >= 7) mfv<s == 0>(sv[t][qt - 7], kr[n * 4 + t], bf[idx]);
      else if constexpr (unit < K_AGPR_UNITS) mfaa<areg(4 * G + t, qt), kreg(unit, s & 3), s == 0>(bf[idx]);
      else mfa<areg(4 * G + t, qt), s == 0>(kr[n * 4 + t], bf[idx]);
      SB();
    });
    MFMA_DRAIN();
  }
  const int lane2 = fresh_lane();
  const int i2 = lane2 & 15, g2 = lane2 >> 4, l16b = lane2 * 16;
  float ls[QT];
  auto expo = [&](auto LOADS) {
    v4u pk_prev = {0, 0, 0, 0};
    sfor<0, QT>([&](auto I) {
      constexpr int pos = decltype(I)::v, qt = pos;
      f32x2 sum0 = {0.f, 0.f}, sum1 = {0.f, 0.f};
      sfor<0, 4>([&](auto U) { constexpr int u = decltype(U)::v; constexpr bool ina = (u < 2 || qt < 7);
        f32x2 p[4];
        if constexpr (ina) rd_tiles_a<areg(2 * u, qt), areg(2 * u + 1, qt)>(p);
        else rd_tiles_v(p, sv[2 * u - 4][qt - 7], sv[2 * u - 3][qt - 7]);
#pragma unroll
        for (int r = 0; r < 4; ++r) {
          const f32x2 z = __builtin_elementwise_fma(p[r], (f32x2){cs, cs}, (f32x2){nm[qt], nm[qt]});
          p[r] = (f32x2){__builtin_amdgcn_exp2f(z[0]), __builtin_amdgcn_exp2f(z[1])};
        }
        asm volatile("" :: "v"(pk_prev));
        sum0 += p[0]; sum1 += p[1]; sum0 += p[2]; sum1 += p[3];
        v4u pk = {cvtpk(p[0][0], p[0][1]), cvtpk(p[1][0], p[1][1]), cvtpk(p[2][0], p[2][1]), cvtpk(p[3][0], p[3][1])};
        lds_wr(lds, ((4 * w + u) * QT + qt) * 1024 + l16b, pk);
        pk_prev = pk;
      });
      sum0 += sum1;
      ls[qt] = sum0[0] + sum0[1];
      if constexpr (decltype(LOADS)::v) sfor<v_cum_sm(pos - 1), v_cum_sm(pos)>([&](auto E) { ldV(E); });
      SB();
    });
  };
  const int ga = h >> 2, ns = h & 3;
  bar();
  expo(IC<1>{});
  {
    float mx = max3f(max3f(ls[0], ls[1], ls[2]), ls[3], ls[4]);
    mx = max3f(max3f(mx, ls[5], ls[6]), ls[7], ls[8]);
    if (mx >= SUM_LIMIT) *(LAS unsigned*)(lds + LDS_FLAG) = 1u;
  }
  bar();
  if (__builtin_expect(__builtin_amdgcn_readfirstlane(*(LAS unsigned*)(lds + LDS_FLAG)) != 0, 0)) {
    sfor<0, QT>([&](auto I) {
      constexpr int qt = decltype(I)::v;
      float m = -INFINITY, m2 = -INFINITY;
      max_tiles_a<areg(0, qt), areg(1, qt)>(m, m2);
      max_tiles_a<areg(2, qt), areg(3, qt)>(m, m2);
      if constexpr (qt < 7) { max_tiles_a<areg(4, qt), areg(5, qt)>(m, m2); max_tiles_a<areg(6, qt), areg(7, qt)>(m, m2); }
      else { max_tiles_v(m, m2, sv[0][qt - 7], sv[1][qt - 7]); max_tiles_v(m, m2, sv[2][qt - 7], sv[3][qt - 7]); }
      *(LAS float*)(lds + LDS_WMAX + qt * 256 + i2 * 16 + w * 4) = xg_max(max3f(m, m2, m2));
      SB();
    });
    bar();
    sfor<0, QT>([&](auto I) {
      constexpr int qt = decltype(I)::v;
      const f32x4 mm = *(LAS f32x4*)(lds + LDS_WMAX + qt * 256 + i2 * 16);
      nm[qt] = -cs * max3f(max3f(mm[0], mm[1], mm[2]), mm[3], mm[3]);
    });
    expo(IC<0>{});
    bar();
  }
  __amdgpu_buffer_rsrc_t rt[4];
#pragma unroll
  for (int st = 0; st < 4; ++st)
    rt[st] = __builtin_amdgcn_make_buffer_rsrc((void*)(ws + WS_WOT + (size_t)((ga * 32 + ((ns + st) & 3) * 8) * 28 + ns * 7 + 2 * w) * 1024), (short)0, (7 * 28 + 2) * 1024, RSRC_W3);
  const int vt1 = w == 3 ? 0x40000000 : l16b + 1024;
  v2u wfl;
  v4u wf[WT_P3 - WT_PRE + 2];
  const __amdgpu_buffer_rsrc_t rf = __builtin_amdgcn_make_buffer_rsrc((void*)(ws + WS_FW + ga * 56 * 8), (short)0, 56 * 8, RSRC_W3);
  auto ldT = [&](auto K) {
    constexpr int k = decltype(K)::v, st = k >> 4, sx = (k >> 1) & 7, j = k & 1;
    if constexpr (k < WT_PRE) ld128_a<144 + 4 * k>(j ? vt1 : l16b, rt[st], sx * 28672);
    else if constexpr (k < WT_P3) ld128_v(wf[k - WT_PRE], j ? vt1 : l16b, rt[st], sx * 28672);
    else ld128_a<72 + 4 * (k - WT_P3)>(j ? vt1 : l16b, rt[st], sx * 28672); };
  {
    v4u bf[16 * QT];
    v4u af[16][4];
    int pb[3] = {l16b, l16b + 65536, l16b + 131072};
    asm volatile("" : "+v"(pb[1]), "+v"(pb[2]));
    auto rdP = [&](auto I) { constexpr int idx = decltype(I)::v; bf[idx] = lds_rd(lds, pb[idx >> 6] + (idx & 63) * 1024); };
    sfor<0, PD>([&](auto I) { rdP(I); });
    wait8<PG.n_v0>(&vr[0]);
    trans4(af[0], &vr[0]);
    SB();
    sfor<0, NS3>([&](auto M) {
      constexpr int m = decltype(M)::v, idx = m >> 2, S = idx / QT, qt = idx % QT, t = m & 3;
      sfor<0, MAXOP>([&](auto K) {
        constexpr Op o = PG.p3[m][decltype(K)::v];
        if constexpr (o.kind == OP_BRD) rdP(IC<o.a>{});
        else if constexpr (o.kind == OP_PERM) af[o.a][o.b >> 2][o.b & 3] = perm1(&vr[o.a * 8], o.b >> 2, o.b & 3);
        else if constexpr (o.kind == OP_VL) ldV(IC<o.a>{});
        else if constexpr (o.kind == OP_WVW) wait8<o.n>(&vr[o.a * 8]);
        else if constexpr (o.kind == OP_WFL) ld_flags(wfl, lane2 * 8, rf);
        else if constexpr (o.kind == OP_WFW) {
          wait_flags<o.n>(wfl);
          for (unsigned spin = 0; spin < SPIN_CAP; ++spin) {
            const bool ok = lane2 >= 56 || (wfl.x == (unsigned)tag && wfl.y == (unsigned)(tag >> 32));
            if (__builtin_expect(__builtin_amdgcn_ballot_w64(ok) == ~0ull, 1)) break;
            __builtin_amdgcn_s_sleep(2);
            ld_flags(wfl, lane2 * 8, rf);
            wait_flags<0>(wfl);
          }
        }
        else if constexpr (o.kind == OP_WTL) ldT(IC<o.a>{});
        else if constexpr (o.kind == OP_SUM) {
          if constexpr (o.a < 2) *(LAS float*)(lds + LDS_WSUM + o.a * 1024 + l16b + w * 4) = xg4<false>(ls[4 * o.a], ls[4 * o.a + 1], ls[4 * o.a + 2], ls[4 * o.a + 3]);
          else *(LAS float*)(lds + LDS_WSUM + 2048 + i2 * 16 + w * 4) = xg_sum(ls[8]);
        }
        else if constexpr (o.kind == OP_LBAR) bar();
      });
      mfa<areg(t, qt), S == 0>(af[S][t], bf[idx]);
      SB();
    });
    const __amdgpu_buffer_rsrc_t ro = __builtin_amdgcn_make_buffer_rsrc((void*)(ws + WS_O + (size_t)bh * 8 * QT * 1024), (short)0, 8 * QT * 1024, RSRC_W3);
    const int so = (2 * w + (g2 >> 1)) * QT * 1024 + (2 * (g2 & 1)) * 256 + i2 * 16;
    v4u b2[4 * QT];
    auto rdP2 = [&](auto I) { constexpr int idx = decltype(I)::v, fi = (12 + (idx & 3)) * QT + (idx >> 2); b2[idx] = lds_rd(lds, pb[fi >> 6] + (fi & 63) * 1024); };
    f32x2 c[8];
    f32x4 ss;
    float rl;
    auto fin = [&](auto Q, auto STEP) {
      constexpr int qt = decltype(Q)::v, st = decltype(STEP)::v;
      if constexpr (st == 0) { ss = *(LAS f32x4*)(lds + LDS_WSUM + qt * 256 + i2 * 16); rd_tiles_a<areg(0, qt), areg(1, qt)>(c); }
      else if constexpr (st == 1) { rd_tiles_a<areg(2, qt), areg(3, qt)>(c + 4); rl = __builtin_amdgcn_rcpf((ss[0] + ss[1]) + (ss[2] + ss[3])); }
      else if constexpr (st == 2) {
        c[0] *= rl; c[2] *= rl; c[4] *= rl; c[6] *= rl;
        const v4u lo = {cvtpk(c[0][0], c[2][0]), cvtpk(c[4][0], c[6][0]), cvtpk(c[0][1], c[2][1]), cvtpk(c[4][1], c[6][1])};
        __builtin_amdgcn_raw_buffer_store_b128(lo, ro, so + qt * 1024, 0, AUX_ST);
        lds_wr(lds, so + qt * 1024, lo);
      } else {
        c[1] *= rl; c[3] *= rl; c[5] *= rl; c[7] *= rl;
        const v4u hi = {cvtpk(c[1][0], c[3][0]), cvtpk(c[5][0], c[7][0]), cvtpk(c[1][1], c[3][1]), cvtpk(c[5][1], c[7][1])};
        __builtin_amdgcn_raw_buffer_store_b128(hi, ro, so + qt * 1024 + 256, 0, AUX_ST);
        lds_wr(lds, so + qt * 1024 + 256, hi);
      }
    };
    sfor<0, PD>([&](auto I) { rdP2(I); });
    SB();
    sfor<0, 4 * QT>([&](auto I) {
      constexpr int idx = decltype(I)::v, qt = idx >> 2, S = 12 + (idx & 3);
      mfa<areg(0, qt), false>(af[S][0], b2[idx]); SB();
      if constexpr (idx + PD < 4 * QT) rdP2(IC<idx + PD>{});
      mfa<areg(1, qt), false>(af[S][1], b2[idx]); SB();
      mfa<areg(2, qt), false>(af[S][2], b2[idx]); SB();
      if constexpr (qt > 0) fin(IC<qt - 1>{}, IC<(idx & 3)>{});
      mfa<areg(3, qt), false>(af[S][3], b2[idx]); SB();
    });
    MFMA_DRAIN();
    sfor<0, 4>([&](auto ST) { fin(IC<QT - 1>{}, ST); });
    SB();
    waitm<18>();
    bar();
  }
  const __amdgpu_buffer_rsrc_t rs = __builtin_amdgcn_make_buffer_rsrc((void*)(ws + WS_SLAB + (size_t)(b * 4 + ns) * 16 * 24 * 1024), (short)0, 16 * 24 * 1024, RSRC_W3);
  const int nj = w == 3 ? 1 : 2;
  auto put_part = [&](auto Q) {
    constexpr int qt = decltype(Q)::v, own = qt < 3 ? 0 : (qt - 1) / 2, slot = qt < 3 ? qt : (qt - 1) & 1;
    if (ga != own) {
      f32x2 c[4];
      rd_tiles_a<8 * qt, 8 * qt + 4>(c);
      const int off = ((own * 4 + ga) * 24 + w * 6 + slot) * 1024;
      const f32x4 t0 = {c[0][0], c[0][1], c[1][0], c[1][1]}, t1 = {c[2][0], c[2][1], c[3][0], c[3][1]};
      __builtin_amdgcn_raw_buffer_store_b128(__builtin_bit_cast(v4u, t0), rs, l16b, off, AUX_ST);
      if (nj == 2) __builtin_amdgcn_raw_buffer_store_b128(__builtin_bit_cast(v4u, t1), rs, l16b, off + 3 * 1024, AUX_ST);
    }
  };
  {
    auto dmaO = [&](int st) {
      const int pbh = b * NH + ga * 4 + ((ns + st) & 3);
      const __amdgpu_buffer_rsrc_t rp = __builtin_amdgcn_make_buffer_rsrc((void*)(ws + WS_O + (size_t)pbh * 8 * QT * 1024), (short)0, 8 * QT * 1024, RSRC_W3);
#pragma unroll
      for (int k = 0; k < 18; ++k) dma1_sc1(lbase + (st & 1) * 73728 + (w * 18 + k) * 1024, l16b, rp, (w * 18 + k) * 1024);
    };
    auto pollO = [&](int st) { poll_flag(ws + WS_FO + (b * NH + ga * 4 + ((ns + st) & 3)) * FSTR + w * WSTR, tag); };
    bool flagged = false;
    auto try_flag = [&](bool force) {
      if (!flagged) {
        unsigned sts;
        asm volatile("s_getreg_b32 %0, hwreg(HW_REG_IB_STS)" : "=s"(sts));
        if (force || (sts & 0x00C0000Fu) == 0) {
          waitm<0>();
          if (lane2 == 0) st_flag(ws + WS_FO + bh * FSTR + w * WSTR, tag);
          { const __amdgpu_buffer_rsrc_t rz = __builtin_amdgcn_make_buffer_rsrc((void*)(ws + WS_O + (size_t)bh * 8 * QT * 1024 + w * 18432), (short)0, 18432, RSRC_W3);
            touch(lbase + LDS_DUMMY, lane2 * 128, rz, 0); touch(lbase + LDS_DUMMY, lane2 * 128, rz, 8192); touch(lbase + LDS_DUMMY, lane2 * 128, rz, 16384); }
          flagged = true;
        }
      }
    };
    auto hop1 = [&]() {
      try_flag(true);
      sfor<WT_EARLY, WT_P3>([&](auto K) { ldT(K); });
      if (w == 0) poll_flags(ws + WS_FO + (b * NH + ga * 4) * FSTR, ((ns + 1 + (lane2 & 1)) & 3) * FSTR + ((lane2 >> 1) & 3) * WSTR, true, tag);
    };
    int ob[3] = {l16b, l16b + 65536, l16b + 131072};
    asm volatile("" : "+v"(ob[1]), "+v"(ob[2]));
    sfor<0, 4>([&](auto ST) {
      constexpr int st = decltype(ST)::v;
      v4u bf[8 * QT];
      auto rdO = [&](auto I) { constexpr int ii = decltype(I)::v, fi = (st & 1) * 72 + (st < 3 ? ii : (ii & 7) * QT + (ii >> 3));
        bf[ii] = lds_rd(lds, ob[fi >> 6] + (fi & 63) * 1024); };
      sfor<0, 4>([&](auto I) { rdO(I); });
      SB();
      sfor<0, 8 * QT>([&](auto I) {
        constexpr int idx = decltype(I)::v, sx = st < 3 ? idx / QT : idx & 7, qt = st < 3 ? idx % QT : idx >> 3;
        constexpr int k = st * 16 + sx * 2;
        if constexpr (st == 3 && sx == 5 && qt > 0) put_part(IC<qt - 1>{});
        if constexpr (st == 0 && qt == 0 && sx < MIDK) try_flag(false);
        if constexpr (st == 0 && qt == 0 && sx == MIDK) { hop1(); asm volatile("s_barrier" ::: "memory"); dmaO(1); }
        if constexpr (st == 1 && (idx & 1) == 0 && idx < 36) ldT(IC<WT_P3 + (idx >> 1)>{});
        if constexpr (k < WT_PRE) mfaa<8 * qt, 144 + 4 * k, st == 0 && sx == 0>(bf[idx]);
        else if constexpr (k < WT_P3) mfa<8 * qt, false>(wf[k - WT_PRE], bf[idx]);
        else mfaa<8 * qt, 72 + 4 * (k - WT_P3), false>(bf[idx]);
        SB();
        if constexpr (idx + 4 < 8 * QT) rdO(IC<idx + 4>{});
        if constexpr (k + 1 < WT_PRE) mfaa<8 * qt + 4, 148 + 4 * k, st == 0 && sx == 0>(bf[idx]);
        else if constexpr (k + 1 < WT_P3) mfa<8 * qt + 4, false>(wf[k + 1 - WT_PRE], bf[idx]);
        else mfaa<8 * qt + 4, 72 + 4 * (k + 1 - WT_P3), false>(bf[idx]);
        SB();
      });
      if constexpr (st == 0) {
        if constexpr (MIDK >= 8) hop1();
        bar();
        if constexpr (MIDK >= 8) dmaO(1);
        dmaO(2);
        waitm<18>();
        wf[18] = wf[19] = (v4u){0, 0, 0, 0};
        sfor<0, 5>([&](auto K) { wait4<18>(&wf[4 * decltype(K)::v]); });
        bar();
      } else if constexpr (st < 3) {
        waitm<0>();
        bar();
        if constexpr (st == 1) { pollO(3); dmaO(3); }
      }
    });
    MFMA_DRAIN();
  }
  {
    const __amdgpu_buffer_rsrc_t rout = __builtin_amdgcn_make_buffer_rsrc((void*)(out + (size_t)b * SQ * DM * 2), (short)0, SQ * DM * 2, RSRC_W3);
    put_part(IC<QT - 1>{});
    waitm<0>();
    if (lane2 == 0) st_flag(ws + WS_FP + bh * FSTR + w * WSTR, tag);
    poll_flags(ws + WS_FP + (b * NH + ns) * FSTR + w * WSTR, (lane2 & 3) * 4 * FSTR, (lane2 & 3) != ga, tag);
    sfor<0, 4>([&](auto A) {
      constexpr int own = decltype(A)::v, q0 = own == 0 ? 0 : 2 * own + 1, nq = own == 0 ? 3 : 2;
      if (ga == own) {
        v4u in[nq][2][3];
#pragma unroll
        for (int sl = 0; sl < nq; ++sl)
#pragma unroll
          for (int e = 0; e < 3; ++e) {
            const int src = e < own ? e : e + 1;
            const int off = ((own * 4 + src) * 24 + w * 6 + sl) * 1024;
            in[sl][0][e] = __builtin_amdgcn_raw_buffer_load_b128(rs, l16b, off, AUX_SC1);
            in[sl][1][e] = __builtin_amdgcn_raw_buffer_load_b128(rs, l16b, off + (nj == 2 ? 3 * 1024 : 0), AUX_SC1);
          }
        SB();
        sfor<0, nq>([&](auto SL) {
          constexpr int sl = decltype(SL)::v, qt = q0 + sl;
          f32x2 c[4];
          rd_tiles_a<8 * qt, 8 * qt + 4>(c);
          f32x4 t[2] = {{c[0][0], c[0][1], c[1][0], c[1][1]}, {c[2][0], c[2][1], c[3][0], c[3][1]}};
          const int oo = ((qt * 16 + i2) * DM + ns * 112 + w * 32 + 4 * g2) * 2;
#pragma unroll
          for (int j = 0; j < 2; ++j) {
            const f32x4 r = (t[j] + __builtin_bit_cast(f32x4, in[sl][j][0])) + (__builtin_bit_cast(f32x4, in[sl][j][1]) + __builtin_bit_cast(f32x4, in[sl][j][2]));
            const v2u o = {cvtpk(r[0], r[1]), cvtpk(r[2], r[3])};
            if (j < nj) __builtin_amdgcn_raw_buffer_store_b64(o, rout, oo + j * 32, 0, AUX_OUT);
          }
        });
      }
    });
  }
  asm volatile(".Lattn_code_end:");
#endif
}

extern "C" void kernel_launch(void* const* d_in, const int* in_sizes, int n_in,
                              void* d_out, int out_size, void* d_ws, size_t ws_size,
                              hipStream_t stream) {
  const char* x = (const char*)d_in[0];
  const char* kc = (const char*)d_in[1];
  const char* vc = (const char*)d_in[2];
  const char* wq = (const char*)d_in[3];
  const char* wo = (const char*)d_in[4];
  char* ws = (char*)d_ws;
  k_attn<<<256, 256, 0, stream>>>(x, kc, vc, wq, wo, ws, (char*)d_out);
}
```
